```python
import torch

def _softmax(x, axis=-1):
    return torch.softmax(x, dim=axis)
def _gelu(x):
    return torch.nn.functional.gelu(x, approximate="tanh")
def _silu(x):
    return torch.nn.functional.silu(x)
def _sigmoid(x):
    return torch.sigmoid(x)
def _layernorm(x, axis=-1, eps=1e-5):
    m = x.mean(dim=axis, keepdim=True)
    v = x.var(dim=axis, keepdim=True, unbiased=False)
    return (x - m) / torch.sqrt(v + eps)
def _scatter_add(tgt, idx, src, axis=0):
    return tgt.clone().index_add_(axis, idx, src)
def _segment_sum(src, seg, n):
    out = torch.zeros((n,) + src.shape[1:], dtype=src.dtype)
    return out.index_add_(0, seg, src)
def _pad(x, pw):
    flat = tuple(v for pair in reversed(pw) for v in pair)
    return torch.nn.functional.pad(x, flat)

kernel_name = 'concat_heads__sliding_block__sliding_block__prompt'

def setup_inputs():

    _w = (lambda a: (lambda t: t.to(torch.bfloat16) if t.is_floating_point() else t)(torch.as_tensor(a)))
    def _A_setup():
        B, S, D = 1, 2048, 448
        return {
            "x": _w((lambda *s: torch.randn(*s, dtype=torch.bfloat16) * 0.4)(B, S, D)),
            "wqkv": _w((lambda *s: torch.randn(*s, dtype=torch.bfloat16) * 0.4)(D, 3 * D)),
            "wo": _w((lambda *s: torch.randn(*s, dtype=torch.bfloat16) * 0.05)(D, D)),
        }
    _A_in = _A_setup()

    def _B_setup():
        B, S, D = 1, 2048, 448
        return {
            "x": _w((lambda *s: torch.randn(*s, dtype=torch.bfloat16) * 0.4)(B, S, D)),
            "wqkv": _w((lambda *s: torch.randn(*s, dtype=torch.bfloat16) * 0.4)(D, 3 * D)),
            "wo": _w((lambda *s: torch.randn(*s, dtype=torch.bfloat16) * 0.05)(D, D)),
        }
    _B_in = _B_setup()

    return {
        "A_x": _A_in["x"],
        "A_wqkv": _A_in["wqkv"],
        "A_wo": _A_in["wo"],
        "B_x": _B_in["x"],
        "B_wqkv": _B_in["wqkv"],
        "B_wo": _B_in["wo"],
    }

def reference(A_x, A_wqkv, A_wo, B_x, B_wqkv, B_wo):
    def _A_ref(x, wqkv, wo):
        B, S, D = x.shape
        H = 16; Dh = D // H; W = 256
        qkv = (lambda x, *d: x.permute(*d))((x @ wqkv).reshape(B, S, 3, H, Dh), 2, 0, 3, 1, 4)
        q, k, v = qkv[0], qkv[1], qkv[2]
        scores = torch.einsum("bhqd,bhkd->bhqk", q, k) * (Dh ** -0.5)
        idx = torch.arange(S)
        mask = (idx[:, None] - idx[None, :] >= 0) & (idx[:, None] - idx[None, :] < W)
        scores = torch.where(mask[None, None], scores, -1e9)
        p = _softmax(scores, axis=-1)
        o = torch.einsum("bhqk,bhkd->bhqd", p, v)
        return (lambda x, *d: x.permute(*d))(o, 0, 2, 1, 3).reshape(B, S, D) @ wo

    def _B_ref(x, wqkv, wo):
        B, S, D = x.shape
        H = 16; Dh = D // H; W = 256
        qkv = (lambda x, *d: x.permute(*d))((x @ wqkv).reshape(B, S, 3, H, Dh), 2, 0, 3, 1, 4)
        q, k, v = qkv[0], qkv[1], qkv[2]
        scores = torch.einsum("bhqd,bhkd->bhqk", q, k) * (Dh ** -0.5)
        idx = torch.arange(S)
        mask = (idx[:, None] - idx[None, :] >= 0) & (idx[:, None] - idx[None, :] < W)
        scores = torch.where(mask[None, None], scores, -1e9)
        p = _softmax(scores, axis=-1)
        o = torch.einsum("bhqk,bhkd->bhqd", p, v)
        return (lambda x, *d: x.permute(*d))(o, 0, 2, 1, 3).reshape(B, S, D) @ wo

    a = _A_ref(A_x, A_wqkv, A_wo)
    b = _B_ref(B_x, B_wqkv, B_wo)
    return torch.concatenate([a, b], axis=-1)
```

```cpp
#include <hip/hip_runtime.h>
#include <hip/hip_bf16.h>
#include <stdint.h>

typedef __bf16 bf16x8 __attribute__((ext_vector_type(8)));
typedef __bf16 bf16x2 __attribute__((ext_vector_type(2)));
typedef float f32x16 __attribute__((ext_vector_type(16)));
typedef float f32x2 __attribute__((ext_vector_type(2)));
typedef short s16x4 __attribute__((ext_vector_type(4)));
typedef short s16x8 __attribute__((ext_vector_type(8)));
typedef unsigned u32x4 __attribute__((ext_vector_type(4)));
typedef unsigned u32x2 __attribute__((ext_vector_type(2)));
#define LAS __attribute__((address_space(3)))
#define GAS __attribute__((address_space(1)))
#define DI __device__ __forceinline__
#define MFMA(a, b, c) __builtin_amdgcn_mfma_f32_32x32x16_bf16((a), (b), (c), 0, 0, 0)

constexpr int S_ = 2048, D_ = 448, NH = 16, DH = 28;
constexpr int XROW = D_ * 2;
constexpr int WROW = 3 * D_ * 2;
constexpr int OROW = 2 * D_ * 2;
constexpr int NTILE = S_ / 32;
constexpr size_t FRAG_TILE = 2048;
constexpr size_t FRAG_ARR = (size_t)2 * NH * NTILE * FRAG_TILE;
constexpr size_t WS_Q = 0, WS_K = FRAG_ARR, WS_V = 2 * FRAG_ARR, WS_O = 3 * FRAG_ARR;

constexpr float SCALE_LOG2E = 0.18898223650461362f * 1.4426950408889634f;

DI unsigned pk2(float a, float b) {
    f32x2 p = {a, b};
    return __builtin_bit_cast(unsigned, __builtin_convertvector(p, bf16x2));
}
DI bf16x8 pack8(const f32x16& x, int s) {
    u32x4 w = {pk2(x[8 * s + 0], x[8 * s + 1]), pk2(x[8 * s + 2], x[8 * s + 3]), pk2(x[8 * s + 4], x[8 * s + 5]), pk2(x[8 * s + 6], x[8 * s + 7])};
    return __builtin_bit_cast(bf16x8, w);
}
DI void glds16(unsigned voff, const void* sbase, unsigned lds_base) {
    unsigned sv;
    asm volatile("s_mov_b32 %0, m0\n\ts_mov_b32 m0, %3\n\ts_nop 0\n\tglobal_load_lds_dwordx4 %1, %2\n\ts_mov_b32 m0, %0"
                 : "=&s"(sv) : "v"(voff), "s"(sbase), "s"(lds_base) : "memory");
}
DI bf16x8 lds_b128(unsigned addr) { return *(const LAS bf16x8*)(size_t)addr; }
DI s16x4 lds_tr(unsigned addr) { return __builtin_amdgcn_ds_read_tr16_b64_v4i16((LAS s16x4*)(size_t)addr); }
DI bf16x8 tr_frag(unsigned a0, unsigned a1) {
    s16x4 lo = lds_tr(a0), hi = lds_tr(a1);
    s16x8 v = __builtin_shufflevector(lo, hi, 0, 1, 2, 3, 4, 5, 6, 7);
    return __builtin_bit_cast(bf16x8, v);
}
#define WAITV_BAR(N) asm volatile("s_waitcnt vmcnt(" #N ") lgkmcnt(0)\n\ts_barrier" ::: "memory")

DI void decode_wg(int bid, int& b, int& r, int& g) {
    const int x = bid & 7, i = bid >> 3;
    b = x >> 2;
    r = ((x >> 1) & 1) * 8 + (i >> 2);
    g = (x & 1) * 4 + (i & 3);
}

constexpr int STG = 40960;
__global__ __launch_bounds__(512, 2) void k_qkv(const char* __restrict__ xA, const char* __restrict__ wA,
                                                const char* __restrict__ xB, const char* __restrict__ wB, char* __restrict__ ws) {
    extern __shared__ __attribute__((aligned(16))) char lds[];
    int b, r, g; decode_wg(blockIdx.x, b, r, g);
    const int tid = threadIdx.x, lane = tid & 63;
    const int w = __builtin_amdgcn_readfirstlane(tid >> 6);
    const int hh = w >> 2, t = w & 3;
    const char* xb = (b ? xB : xA) + (size_t)r * 128 * XROW;
    const char* wb = (b ? wB : wA) + g * 112;
    const unsigned lbase = (unsigned)(size_t)lds;

    const unsigned xo0 = (unsigned)((16 * w + (lane >> 3)) * XROW + (((lane & 7) ^ (lane >> 4)) << 4));
    const unsigned xo1 = (unsigned)((16 * w + 8 + (lane >> 3)) * XROW + (((lane & 7) ^ (4 + (lane >> 4))) << 4));
    int wc = (lane & 7) ^ (((lane >> 4) & 1) << 2); wc = wc > 6 ? 6 : wc;
    const unsigned wo0 = (unsigned)((8 * w + (lane >> 3)) * WROW + wc * 16);

    auto issue = [&](int s) {
        const unsigned sb = lbase + (s & 3) * STG;
        glds16(xo0 + s * 128, xb, sb + (16 * w) * 128);
        glds16(xo1 + s * 128, xb, sb + (16 * w + 8) * 128);
#pragma unroll
        for (int p = 0; p < 3; ++p) glds16(wo0 + s * 64 * WROW + p * 896, wb, sb + 16384 + p * 8192 + (8 * w) * 128);
    };
    issue(0); issue(1); issue(2); issue(3);

    const int r31 = lane & 31, h = lane >> 5;
    const int xrow = 32 * t + r31;
    const unsigned xa = (unsigned)(xrow * 128 + ((h ^ ((xrow >> 1) & 7)) << 4));
    const int blk = (lane >> 4) & 1, q = (lane & 15) >> 2, pp = lane & 3;
    const unsigned wa = (unsigned)(16384 + (8 * h + q) * 128 + ((56 * hh + 32 * blk + 8 * pp) ^ (((q >> 1) & 1) << 6)));

    f32x16 aq = {}, ak = {}, av = {};
#pragma unroll
    for (int s = 0; s < 7; ++s) {
        if (s == 0) WAITV_BAR(15);
        else if (s <= 4) WAITV_BAR(10);
        else if (s == 5) WAITV_BAR(5);
        else WAITV_BAR(0);
        if (s >= 1 && s <= 3) issue(s + 3);
        const unsigned sb = lbase + (s & 3) * STG;
#pragma unroll
        for (int ks = 0; ks < 4; ++ks) {
            const bf16x8 xf = lds_b128(sb + (xa ^ (ks << 5)));
            const unsigned wbase = sb + wa + ks * 16 * 128;
            const bf16x8 wq = tr_frag(wbase, wbase + 512);
            const bf16x8 wk = tr_frag(wbase + 8192, wbase + 8192 + 512);
            const bf16x8 wv = tr_frag(wbase + 16384, wbase + 16384 + 512);
            aq = MFMA(wq, xf, aq);
            ak = MFMA(wk, xf, ak);
            av = MFMA(xf, wv, av);
        }
    }
    if (h == 1) {
#pragma unroll
        for (int i = 12; i < 16; ++i) { aq[i] = 0.f; ak[i] = 0.f; }
    }
    if (r31 >= DH) {
        const float vv = (r31 == DH) ? 1.f : 0.f;
#pragma unroll
        for (int i = 0; i < 16; ++i) av[i] = vv;
    }
    const size_t fo = ((size_t)((b * NH + 2 * g + hh) * NTILE + 4 * r + t)) * FRAG_TILE + lane * 16;
#pragma unroll
    for (int s = 0; s < 2; ++s) {
        *(bf16x8*)(ws + WS_Q + fo + s * 1024) = pack8(aq, s);
        *(bf16x8*)(ws + WS_K + fo + s * 1024) = pack8(ak, s);
        *(bf16x8*)(ws + WS_V + fo + s * 1024) = pack8(av, s);
    }
}

__global__ __launch_bounds__(512, 2) void k_attn(char* __restrict__ ws) {
    extern __shared__ __attribute__((aligned(16))) char lds[];
    int b, r, g; decode_wg(blockIdx.x, b, r, g);
    const int tid = threadIdx.x, lane = tid & 63;
    const int w = __builtin_amdgcn_readfirstlane(tid >> 6);
    const int hh = w >> 2, t = w & 3;
    const unsigned lbase = (unsigned)(size_t)lds;
    const int T0 = 4 * r - 8;
#pragma unroll
    for (int u = 0; u < 3; ++u) {
        const int unit = 3 * w + u, uh = unit / 12, j = unit % 12;
        const int T = T0 + j;
        if (T >= 0) {
            const size_t fo = ((size_t)((b * NH + 2 * g + uh) * NTILE + T)) * FRAG_TILE;
            const unsigned dst = lbase + unit * 4096;
            glds16(lane * 16, ws + WS_K + fo, dst);
            glds16(lane * 16 + 1024, ws + WS_K + fo, dst + 1024);
            glds16(lane * 16, ws + WS_V + fo, dst + 2048);
            glds16(lane * 16 + 1024, ws + WS_V + fo, dst + 3072);
        }
    }
    const size_t qo = ((size_t)((b * NH + 2 * g + hh) * NTILE + 4 * r + t)) * FRAG_TILE + lane * 16;
    const bf16x8 q0 = *(const bf16x8*)(ws + WS_Q + qo), q1 = *(const bf16x8*)(ws + WS_Q + qo + 1024);
    WAITV_BAR(0);

    const int a = lane & 31, h = lane >> 5;
    const unsigned kb = lbase + (hh * 12 + t) * 4096 + lane * 16;
    f32x16 sc[9];
    const float NEG = -__builtin_inff();
#pragma unroll
    for (int j = 0; j < 9; ++j) {
        if (T0 + t + j >= 0) {
            const bf16x8 k0 = lds_b128(kb + j * 4096), k1 = lds_b128(kb + j * 4096 + 1024);
            f32x16 z = {};
            z = MFMA(k0, q0, z);
            sc[j] = MFMA(k1, q1, z);
        } else {
#pragma unroll
            for (int i = 0; i < 16; ++i) sc[j][i] = NEG;
        }
    }
#pragma unroll
    for (int i = 0; i < 16; ++i) {
        const int c = (i & 3) + 8 * (i >> 2) + 4 * h;
        if (c <= a) sc[0][i] = NEG;
        if (c > a) sc[8][i] = NEG;
    }
    float m = NEG;
#pragma unroll
    for (int j = 0; j < 9; ++j)
#pragma unroll
        for (int i = 0; i < 16; ++i) m = __builtin_fmaxf(m, sc[j][i]);
    {
        auto rr = __builtin_amdgcn_permlane32_swap(__float_as_uint(m), __float_as_uint(m), false, false);
        m = __builtin_fmaxf(__uint_as_float(rr[0]), __uint_as_float(rr[1]));
    }
    const float nm = -m * SCALE_LOG2E;
    f32x16 o = {};
#pragma unroll
    for (int j = 0; j < 9; ++j) {
        if (T0 + t + j >= 0) {
#pragma unroll
            for (int i = 0; i < 16; ++i) sc[j][i] = __builtin_amdgcn_exp2f(__builtin_fmaf(sc[j][i], SCALE_LOG2E, nm));
            const bf16x8 p0 = pack8(sc[j], 0), p1 = pack8(sc[j], 1);
            const bf16x8 v0 = lds_b128(kb + j * 4096 + 2048), v1 = lds_b128(kb + j * 4096 + 3072);
            o = MFMA(v0, p0, o);
            o = MFMA(v1, p1, o);
        }
    }
    float l = o[12];
    {
        auto rr = __builtin_amdgcn_permlane32_swap(__float_as_uint(l), __float_as_uint(l), false, false);
        l = __uint_as_float(rr[1]);
    }
    const float inv = 1.0f / l;
#pragma unroll
    for (int i = 0; i < 16; ++i) o[i] *= inv;
    if (h == 1) {
#pragma unroll
        for (int i = 12; i < 16; ++i) o[i] = 0.f;
    }
    *(bf16x8*)(ws + WS_O + qo) = pack8(o, 0);
    *(bf16x8*)(ws + WS_O + qo + 1024) = pack8(o, 1);
}

__global__ __launch_bounds__(512, 2) void k_oproj(const char* __restrict__ woA, const char* __restrict__ woB,
                                                  const char* __restrict__ ws, char* __restrict__ out) {
    extern __shared__ __attribute__((aligned(16))) char lds[];
    int b, r, g; decode_wg(blockIdx.x, b, r, g);
    const int tid = threadIdx.x, lane = tid & 63;
    const int w = __builtin_amdgcn_readfirstlane(tid >> 6);
    const int kh = w >> 2, t = w & 3;
    const unsigned lbase = (unsigned)(size_t)lds;
    const char* wb = (b ? woB : woA) + g * 112;
    int wc = (lane & 7) ^ (((lane >> 4) & 1) << 2); wc = wc > 6 ? 6 : wc;
#pragma unroll
    for (int i = 0; i < 8; ++i) {
        const int piece = w * 7 + i;
        if (i < 7 || w == 7) {
            int row = piece * 8 + (lane >> 3); row = row > 447 ? 447 : row;
            glds16((unsigned)(row * XROW + wc * 16), wb, lbase + piece * 1024);
        }
    }
    bf16x8 of[16];
    const size_t oo = ((size_t)((b * NH + 8 * kh) * NTILE + 4 * r + t)) * FRAG_TILE + lane * 16;
#pragma unroll
    for (int i = 0; i < 8; ++i) {
        of[2 * i] = *(const bf16x8*)(ws + WS_O + oo + (size_t)i * NTILE * FRAG_TILE);
        of[2 * i + 1] = *(const bf16x8*)(ws + WS_O + oo + (size_t)i * NTILE * FRAG_TILE + 1024);
    }
    WAITV_BAR(0);
    const int r31 = lane & 31, h = lane >> 5;
    const int blk = (lane >> 4) & 1, q = (lane & 15) >> 2, pp = lane & 3;
    const unsigned wa = lbase + (unsigned)((8 * kh * DH + 4 * h + q) * 128);
    const unsigned c0 = (unsigned)((32 * blk + 8 * pp) ^ (((q >> 1) & 1) << 6));
    const unsigned c1 = (unsigned)((64 + 32 * blk + 8 * pp) ^ (((q >> 1) & 1) << 6));
    f32x16 a0 = {}, a1 = {};
#pragma unroll
    for (int i = 0; i < 8; ++i)
#pragma unroll
        for (int s = 0; s < 2; ++s) {
            const unsigned rb = wa + (i * DH + 16 * s) * 128;
            const bf16x8 w0 = tr_frag(rb + c0, rb + c0 + 8 * 128);
            const bf16x8 w1 = tr_frag(rb + c1, rb + c1 + 8 * 128);
            a0 = MFMA(w0, of[2 * i + s], a0);
            a1 = MFMA(w1, of[2 * i + s], a1);
        }
    __syncthreads();
    float* xch = (float*)lds;
    {
        const f32x16& give = kh ? a0 : a1;
        float* dst = xch + ((kh ^ 1) * 4 + t) * 1024 + lane * 4;
#pragma unroll
        for (int i = 0; i < 4; ++i) *(float4*)(dst + i * 256) = make_float4(give[4 * i], give[4 * i + 1], give[4 * i + 2], give[4 * i + 3]);
    }
    __syncthreads();
    f32x16 acc = kh ? a1 : a0;
    {
        const float* src = xch + (kh * 4 + t) * 1024 + lane * 4;
#pragma unroll
        for (int i = 0; i < 4; ++i) { const float4 v = *(const float4*)(src + i * 256); acc[4 * i] += v.x; acc[4 * i + 1] += v.y; acc[4 * i + 2] += v.z; acc[4 * i + 3] += v.w; }
    }
    char* orow = out + (size_t)(128 * r + 32 * t + r31) * OROW + (b * D_ + 56 * g + 32 * kh) * 2;
#pragma unroll
    for (int qq = 0; qq < 4; qq += 2) {
        unsigned ax = pk2(acc[4 * qq], acc[4 * qq + 1]), ay = pk2(acc[4 * qq + 2], acc[4 * qq + 3]);
        unsigned bx = pk2(acc[4 * qq + 4], acc[4 * qq + 5]), by = pk2(acc[4 * qq + 6], acc[4 * qq + 7]);
        auto r0 = __builtin_amdgcn_permlane32_swap(ax, bx, false, false);
        auto r1 = __builtin_amdgcn_permlane32_swap(ay, by, false, false);
        u32x4 v = {r0[0], r1[0], r0[1], r1[1]};
        const int n0 = 8 * qq + 8 * h;
        if (32 * kh + n0 < 56) *(u32x4*)(orow + n0 * 2) = v;
    }
}

extern "C" void kernel_launch(void* const* d_in, const int* in_sizes, int n_in, void* d_out, int out_size, void* d_ws, size_t ws_size,
                              hipStream_t stream) {
    const char* xA = (const char*)d_in[0]; const char* wA = (const char*)d_in[1]; const char* woA = (const char*)d_in[2];
    const char* xB = (const char*)d_in[3]; const char* wB = (const char*)d_in[4]; const char* woB = (const char*)d_in[5];
    char* ws = (char*)d_ws;
    hipFuncSetAttribute((const void*)k_qkv, hipFuncAttributeMaxDynamicSharedMemorySize, 4 * STG);
    hipFuncSetAttribute((const void*)k_attn, hipFuncAttributeMaxDynamicSharedMemorySize, 24 * 4096);
    hipFuncSetAttribute((const void*)k_oproj, hipFuncAttributeMaxDynamicSharedMemorySize, 57 * 1024);
    k_qkv<<<256, 512, 4 * STG, stream>>>(xA, wA, xB, wB, ws);
    k_attn<<<256, 512, 24 * 4096, stream>>>(ws);
    k_oproj<<<256, 512, 57 * 1024, stream>>>(woA, woB, (const char*)ws, (char*)d_out);
}
```

```cpp
#include <hip/hip_runtime.h>
#include <hip/hip_bf16.h>
#include <stdint.h>

typedef __bf16 bf16x8 __attribute__((ext_vector_type(8)));
typedef __bf16 bf16x2 __attribute__((ext_vector_type(2)));
typedef float f32x16 __attribute__((ext_vector_type(16)));
typedef float f32x2 __attribute__((ext_vector_type(2)));
typedef short s16x4 __attribute__((ext_vector_type(4)));
typedef short s16x8 __attribute__((ext_vector_type(8)));
typedef unsigned u32x4 __attribute__((ext_vector_type(4)));
typedef unsigned u32x2 __attribute__((ext_vector_type(2)));
typedef unsigned long long u64;
#define LAS __attribute__((address_space(3)))
#define GAS __attribute__((address_space(1)))
#define DI __device__ __forceinline__
#define MFMA(a, b, c) __builtin_amdgcn_mfma_f32_32x32x16_bf16((a), (b), (c), 0, 0, 0)
#define RLX_AGENT __ATOMIC_RELAXED, __HIP_MEMORY_SCOPE_AGENT

constexpr int S_ = 2048, D_ = 448, NH = 16, DH = 28;
constexpr int XROW = D_ * 2;
constexpr int WROW = 3 * D_ * 2;
constexpr int OROW = 2 * D_ * 2;
constexpr int NTILE = S_ / 32;
constexpr unsigned WS_G = 0;
constexpr unsigned WS_F1 = 4096;
constexpr unsigned WS_F2 = WS_F1 + 256 * 128;
constexpr unsigned WS_KV = 1u << 20;
constexpr unsigned KV_BYTES = 2u * NH * NTILE * 4096;
constexpr unsigned WS_O = WS_KV + KV_BYTES;
constexpr unsigned O_BYTES = 2u * NTILE * NH * 2048;
constexpr unsigned WS_END = WS_O + O_BYTES;
constexpr float SCALE_LOG2E = 0.18898223650461362f * 1.4426950408889634f;
constexpr int AUX_SC1 = 16;

DI unsigned pk2(float a, float b) {
    f32x2 p = {a, b};
    return __builtin_bit_cast(unsigned, __builtin_convertvector(p, bf16x2));
}
DI u32x4 pack8u(const f32x16& x, int s) {
    u32x4 w = {pk2(x[8 * s + 0], x[8 * s + 1]), pk2(x[8 * s + 2], x[8 * s + 3]), pk2(x[8 * s + 4], x[8 * s + 5]), pk2(x[8 * s + 6], x[8 * s + 7])};
    return w;
}
DI bf16x8 pack8(const f32x16& x, int s) { return __builtin_bit_cast(bf16x8, pack8u(x, s)); }
DI void glds16(unsigned voff, const void* sbase, unsigned lds_base) {
    unsigned sv;
    asm volatile("s_mov_b32 %0, m0\n\ts_mov_b32 m0, %3\n\ts_nop 0\n\tglobal_load_lds_dwordx4 %1, %2\n\ts_mov_b32 m0, %0"
                 : "=&s"(sv) : "v"(voff), "s"(sbase), "s"(lds_base) : "memory");
}
DI bf16x8 lds_b128(unsigned addr) { return *(const LAS bf16x8*)(size_t)addr; }
DI void lds_w128(unsigned addr, u32x4 v) { *(LAS u32x4*)(size_t)addr = v; }
DI s16x4 lds_tr(unsigned addr) { return __builtin_amdgcn_ds_read_tr16_b64_v4i16((LAS s16x4*)(size_t)addr); }
DI bf16x8 cat8(s16x4 lo, s16x4 hi) { s16x8 v = __builtin_shufflevector(lo, hi, 0, 1, 2, 3, 4, 5, 6, 7); return __builtin_bit_cast(bf16x8, v); }
DI bf16x8 tr_frag(unsigned a0, unsigned a1) { return cat8(lds_tr(a0), lds_tr(a1)); }

#define STAMP(i)
#define WAITV_BAR(N) asm volatile("s_waitcnt vmcnt(" #N ") lgkmcnt(0)\n\ts_barrier" ::: "memory")
#define BAR() asm volatile("s_barrier" ::: "memory")
#define LBAR() asm volatile("s_waitcnt lgkmcnt(0)\n\ts_barrier" ::: "memory")

DI void wait_flags(const char* ws, unsigned off, bool active, u64 e) {
    const GAS u64* p = (const GAS u64*)(ws + off);
    const u64 t0 = __builtin_amdgcn_s_memrealtime();
    for (;;) {
        u64 v = e;
        if (active) v = __hip_atomic_load(p, RLX_AGENT);
        if (__all(v == e)) break;
        __builtin_amdgcn_s_sleep(1);
        if (__builtin_amdgcn_s_memrealtime() - t0 > 2000000ull) break;
    }
    asm volatile("" ::: "memory");
}

constexpr int STG = 40960;
constexpr int LDS_BYTES = 4 * STG;
constexpr int L_KV = 0;
constexpr int L_WO0 = 98304;
constexpr int L_WO1 = 0;
constexpr int L_XCH = 58368;

#define ROT 1

__global__ __launch_bounds__(512, 2) void k_fused(const char* __restrict__ xA, const char* __restrict__ wA, const char* __restrict__ woA,
                                                  const char* __restrict__ xB, const char* __restrict__ wB, const char* __restrict__ woB,
                                                  char* __restrict__ out, char* __restrict__ ws) {
    extern __shared__ __attribute__((aligned(16))) char lds[];
    const u64 epoch = *(const u64*)(ws + WS_G) + 1ull;
    STAMP(0);
    const int xcd = blockIdx.x & 7, idx = blockIdx.x >> 3;
    const int b = xcd >> 2;
    const int r = ((xcd >> 1) & 1) * 8 + (idx >> 2);
    const int g = (xcd & 1) * 4 + (idx & 3);
    const int wg = (b * 16 + r) * 8 + g;
    const int tid = threadIdx.x, lane = tid & 63;
    const int w = __builtin_amdgcn_readfirstlane(tid >> 6);
    const int hh = w >> 2, t = w & 3;
    const char* xb = (b ? xB : xA) + (size_t)r * 128 * XROW;
    const char* wb = (b ? wB : wA) + g * 112;
    const unsigned lbase = (unsigned)(size_t)lds;
    const __amdgpu_buffer_rsrc_t rs = __builtin_amdgcn_make_buffer_rsrc(ws, 0, WS_END, 0x00020000);

    const unsigned xo0 = (unsigned)((16 * w + (lane >> 3)) * XROW + (((lane & 7) ^ (lane >> 4)) << 4));
    const unsigned xo1 = (unsigned)((16 * w + 8 + (lane >> 3)) * XROW + (((lane & 7) ^ (4 + (lane >> 4))) << 4));
    int wc = (lane & 7) ^ (((lane >> 4) & 1) << 2); wc = wc > 6 ? 6 : wc;
    const unsigned wo0 = (unsigned)((8 * w + (lane >> 3)) * WROW + wc * 16);
    const int rot = ROT ? (2 * (r & 7) + 3 * (g & 3)) % 7 : 0;

    auto issue = [&](int s0) {
        int s = s0 + rot; s = s >= 7 ? s - 7 : s;
        const unsigned sb = lbase + (s0 & 3) * STG;
        glds16(xo0 + s * 128, xb, sb + (16 * w) * 128);
        glds16(xo1 + s * 128, xb, sb + (16 * w + 8) * 128);
#pragma unroll
        for (int p = 0; p < 3; ++p) glds16(wo0 + s * 64 * WROW + p * 896, wb, sb + 16384 + p * 8192 + (8 * w) * 128);
    };
    issue(0); BAR(); issue(1); BAR(); issue(2); BAR(); issue(3);

    const int r31 = lane & 31, h = lane >> 5;
    const int xrow = 32 * t + r31;
    const unsigned xa = (unsigned)(xrow * 128 + ((h ^ ((xrow >> 1) & 7)) << 4));
    const int blk = (lane >> 4) & 1, q = (lane & 15) >> 2, pp = lane & 3;
    const unsigned wsw = ((q >> 1) & 1) << 6;
    const unsigned wa = (unsigned)(16384 + (8 * h + q) * 128 + ((56 * hh + 32 * blk + 8 * pp) ^ wsw));

    f32x16 aq = {}, ak = {}, av = {};
#pragma unroll
    for (int s = 0; s < 7; ++s) {
        if (s == 0) WAITV_BAR(15);
        else if (s <= 4) WAITV_BAR(10);
        else if (s == 5) WAITV_BAR(5);
        else WAITV_BAR(0);
        STAMP(1 + s);
        if (s >= 1 && s <= 3) issue(s + 3);
        const unsigned sb = lbase + (s & 3) * STG;
#pragma unroll
        for (int ks = 0; ks < 4; ++ks) {
            const bf16x8 xf = lds_b128(sb + (xa ^ (ks << 5)));
            const unsigned wbase = sb + wa + ks * 16 * 128;
            const bf16x8 wq = tr_frag(wbase, wbase + 512);
            const bf16x8 wk = tr_frag(wbase + 8192, wbase + 8192 + 512);
            const bf16x8 wv = tr_frag(wbase + 16384, wbase + 16384 + 512);
            aq = MFMA(wq, xf, aq);
            ak = MFMA(wk, xf, ak);
            av = MFMA(xf, wv, av);
        }
    }
    STAMP(8);
    if (h == 1) {
#pragma unroll
        for (int i = 12; i < 16; ++i) { aq[i] = 0.f; ak[i] = 0.f; }
    }
    if (r31 >= DH) {
        const float vv = (r31 == DH) ? 1.f : 0.f;
#pragma unroll
        for (int i = 0; i < 16; ++i) av[i] = vv;
    }
    const bf16x8 q0 = pack8(aq, 0), q1 = pack8(aq, 1);
    {
        const u32x4 k0 = pack8u(ak, 0), k1 = pack8u(ak, 1), v0 = pack8u(av, 0), v1 = pack8u(av, 1);
        const unsigned go = WS_KV + (unsigned)(((b * NH + 2 * g + hh) * NTILE + 4 * r + t) * 4096) + lane * 16;
        __builtin_amdgcn_raw_buffer_store_b128(k0, rs, go, 0, AUX_SC1);
        __builtin_amdgcn_raw_buffer_store_b128(k1, rs, go + 1024, 0, AUX_SC1);
        __builtin_amdgcn_raw_buffer_store_b128(v0, rs, go + 2048, 0, AUX_SC1);
        __builtin_amdgcn_raw_buffer_store_b128(v1, rs, go + 3072, 0, AUX_SC1);
        LBAR();
        const unsigned lo = lbase + L_KV + (hh * 12 + 8 + t) * 4096 + lane * 16;
        lds_w128(lo, k0); lds_w128(lo + 1024, k1); lds_w128(lo + 2048, v0); lds_w128(lo + 3072, v1);
    }
    const int ct = g & 3, rb3 = 2 * r + (g >> 2);
    const char* wob = (b ? woB : woA) + ct * 256;
    const int wc3 = (lane & 7) ^ (((lane >> 4) & 1) << 2);
    asm volatile("s_waitcnt vmcnt(0)" ::: "memory");
    LBAR();
    if (tid == 0) __hip_atomic_store((GAS u64*)(ws + WS_F1 + wg * 128), epoch, RLX_AGENT);
    STAMP(9);
#pragma unroll
    for (int i = 0; i < 8; ++i) {
        const int piece = w * 7 + i;
        if (i < 7 || w == 7) {
            int row = piece * 8 + (lane >> 3); row = row > 447 ? 447 : row;
            glds16((unsigned)(row * XROW + wc3 * 16), wob, lbase + L_WO0 + piece * 1024);
        }
    }

    if (w == 0) wait_flags(ws, WS_F1 + (wg - 8 * (1 + (lane & 1))) * 128, lane < 2 && r - 1 - (lane & 1) >= 0, epoch);
    BAR();
    STAMP(10);
    {
        u32x4 v[8];
#pragma unroll
        for (int u = 0; u < 2; ++u) {
            const int unit = 2 * w + u, uh = unit >> 3, j = unit & 7, T = 4 * r - 8 + j;
            const unsigned go = WS_KV + (unsigned)(((b * NH + 2 * g + uh) * NTILE + (T < 0 ? 0 : T)) * 4096) + lane * 16;
            if (T >= 0) {
#pragma unroll
                for (int k = 0; k < 4; ++k) v[4 * u + k] = __builtin_amdgcn_raw_buffer_load_b128(rs, go + k * 1024, 0, AUX_SC1);
            }
        }
#pragma unroll
        for (int u = 0; u < 2; ++u) {
            const int unit = 2 * w + u, uh = unit >> 3, j = unit & 7, T = 4 * r - 8 + j;
            if (T >= 0) {
                const unsigned lo = lbase + L_KV + (uh * 12 + j) * 4096 + lane * 16;
#pragma unroll
                for (int k = 0; k < 4; ++k) lds_w128(lo + k * 1024, v[4 * u + k]);
            }
        }
    }
    LBAR();
    STAMP(11);
    const int T0 = 4 * r - 8;
    const unsigned kb = lbase + L_KV + (hh * 12 + t) * 4096 + lane * 16;
    f32x16 sc[9];
    const float NEG = -__builtin_inff();
#pragma unroll
    for (int j = 0; j < 9; ++j) {
        if (T0 + t + j >= 0) {
            const bf16x8 k0 = lds_b128(kb + j * 4096), k1 = lds_b128(kb + j * 4096 + 1024);
            f32x16 z = {};
            z = MFMA(k0, q0, z);
            sc[j] = MFMA(k1, q1, z);
        } else {
#pragma unroll
            for (int i = 0; i < 16; ++i) sc[j][i] = NEG;
        }
    }
#pragma unroll
    for (int i = 0; i < 16; ++i) {
        const int c = (i & 3) + 8 * (i >> 2) + 4 * h;
        if (c <= r31) sc[0][i] = NEG;
        if (c > r31) sc[8][i] = NEG;
    }
    float m = NEG;
#pragma unroll
    for (int j = 0; j < 9; ++j)
#pragma unroll
        for (int i = 0; i < 16; ++i) m = __builtin_fmaxf(m, sc[j][i]);
    {
        auto rr = __builtin_amdgcn_permlane32_swap(__float_as_uint(m), __float_as_uint(m), false, false);
        m = __builtin_fmaxf(__uint_as_float(rr[0]), __uint_as_float(rr[1]));
    }
    STAMP(12);
    const float nm = -m * SCALE_LOG2E;
    f32x16 o = {};
#pragma unroll
    for (int j = 0; j < 9; ++j) {
        if (T0 + t + j >= 0) {
#pragma unroll
            for (int i = 0; i < 16; ++i) sc[j][i] = __builtin_amdgcn_exp2f(__builtin_fmaf(sc[j][i], SCALE_LOG2E, nm));
            const bf16x8 p0 = pack8(sc[j], 0), p1 = pack8(sc[j], 1);
            const bf16x8 v0 = lds_b128(kb + j * 4096 + 2048), v1 = lds_b128(kb + j * 4096 + 3072);
            o = MFMA(v0, p0, o);
            o = MFMA(v1, p1, o);
        }
    }
    STAMP(13);
    float l = o[12];
    {
        auto rr = __builtin_amdgcn_permlane32_swap(__float_as_uint(l), __float_as_uint(l), false, false);
        l = __uint_as_float(rr[1]);
    }
    const float inv = 1.0f / l;
#pragma unroll
    for (int i = 0; i < 16; ++i) o[i] *= inv;
    if (h == 1) {
#pragma unroll
        for (int i = 12; i < 16; ++i) o[i] = 0.f;
    }
    {
        const unsigned go = WS_O + (unsigned)(((b * NTILE + 4 * r + t) * NH + 2 * g + hh) * 2048) + lane * 16;
        __builtin_amdgcn_raw_buffer_store_b128(pack8u(o, 0), rs, go, 0, AUX_SC1);
        __builtin_amdgcn_raw_buffer_store_b128(pack8u(o, 1), rs, go + 1024, 0, AUX_SC1);
    }
    asm volatile("s_waitcnt vmcnt(0)" ::: "memory");
    LBAR();
    if (tid == 0) __hip_atomic_store((GAS u64*)(ws + WS_F2 + wg * 128), epoch, RLX_AGENT);
    STAMP(14);
    const int nimg = ct == 3 ? 1 : 2;
    if (nimg == 2) {
#pragma unroll
        for (int i = 0; i < 8; ++i) {
            const int piece = w * 7 + i;
            if (i < 7 || w == 7) {
                int row = piece * 8 + (lane >> 3); row = row > 447 ? 447 : row;
                glds16((unsigned)(row * XROW + 128 + wc3 * 16), wob, lbase + L_WO1 + piece * 1024);
            }
        }
    }

    if (w == 0) wait_flags(ws, WS_F2 + ((b * 16 + r) * 8 + (lane & 7)) * 128, lane < 8, epoch);
    BAR();
    STAMP(15);
    const int tt = w & 1, kq = w >> 1;
    bf16x8 of[8];
    {
        const unsigned go = WS_O + (unsigned)(((b * NTILE + 2 * rb3 + tt) * NH + 4 * kq) * 2048) + lane * 16;
#pragma unroll
        for (int i = 0; i < 8; ++i) of[i] = __builtin_bit_cast(bf16x8, __builtin_amdgcn_raw_buffer_load_b128(rs, go + i * 1024, 0, AUX_SC1));
    }
    WAITV_BAR(0);
    STAMP(16);
    f32x16 acc[4] = {};
    {
        const unsigned wrow0 = lbase + L_WO0 + (unsigned)((4 * kq * DH + 4 * h + q) * 128);
        const unsigned wrow1 = lbase + L_WO1 + (unsigned)((4 * kq * DH + 4 * h + q) * 128);
        const unsigned c0 = (32 * blk + 8 * pp) ^ wsw, c1 = (64 + 32 * blk + 8 * pp) ^ wsw;
#pragma unroll
        for (int i = 0; i < 4; ++i)
#pragma unroll
            for (int s = 0; s < 2; ++s) {
                const unsigned ro = (i * DH + 16 * s) * 128;
                acc[0] = MFMA(tr_frag(wrow0 + ro + c0, wrow0 + ro + c0 + 1024), of[2 * i + s], acc[0]);
                acc[1] = MFMA(tr_frag(wrow0 + ro + c1, wrow0 + ro + c1 + 1024), of[2 * i + s], acc[1]);
                if (nimg == 2) {
                    acc[2] = MFMA(tr_frag(wrow1 + ro + c0, wrow1 + ro + c0 + 1024), of[2 * i + s], acc[2]);
                    acc[3] = MFMA(tr_frag(wrow1 + ro + c1, wrow1 + ro + c1 + 1024), of[2 * i + s], acc[3]);
                }
            }
    }
    STAMP(17);
    LBAR();
    float* xch = (float*)lds;
#pragma unroll
    for (int n = 0; n < 4; ++n) {
        if (n != kq) {
            float* dst = xch + ((tt * 4 + n) * 4 + kq) * 1024 + lane * 4;
#pragma unroll
            for (int e = 0; e < 4; ++e) *(float4*)(dst + e * 256) = make_float4(acc[n][4 * e], acc[n][4 * e + 1], acc[n][4 * e + 2], acc[n][4 * e + 3]);
        }
    }
    LBAR();
    f32x16 fin = kq == 0 ? acc[0] : kq == 1 ? acc[1] : kq == 2 ? acc[2] : acc[3];
#pragma unroll
    for (int k = 0; k < 4; ++k) {
        if (k != kq) {
            const float* src = xch + ((tt * 4 + kq) * 4 + k) * 1024 + lane * 4;
#pragma unroll
            for (int e = 0; e < 4; ++e) { const float4 v = *(const float4*)(src + e * 256); fin[4 * e] += v.x; fin[4 * e + 1] += v.y; fin[4 * e + 2] += v.z; fin[4 * e + 3] += v.w; }
        }
    }
    if (kq < 2 * nimg) {
        char* orow = out + (size_t)(64 * rb3 + 32 * tt + r31) * OROW + (b * D_ + 128 * ct + 32 * kq) * 2;
#pragma unroll
        for (int qq = 0; qq < 4; qq += 2) {
            unsigned ax = pk2(fin[4 * qq], fin[4 * qq + 1]), ay = pk2(fin[4 * qq + 2], fin[4 * qq + 3]);
            unsigned bx = pk2(fin[4 * qq + 4], fin[4 * qq + 5]), by = pk2(fin[4 * qq + 6], fin[4 * qq + 7]);
            auto r0 = __builtin_amdgcn_permlane32_swap(ax, bx, false, false);
            auto r1 = __builtin_amdgcn_permlane32_swap(ay, by, false, false);
            u32x4 v = {r0[0], r1[0], r0[1], r1[1]};
            *(u32x4*)(orow + (8 * qq + 8 * h) * 2) = v;
        }
    }
    STAMP(18);
    if (wg == 0 && w == 0) {
        const u64 t0 = __builtin_amdgcn_s_memrealtime();
        for (;;) {
            bool ok = true;
#pragma unroll
            for (int k = 0; k < 4; ++k) ok &= __hip_atomic_load((const GAS u64*)(ws + WS_F2 + (k * 64 + lane) * 128), RLX_AGENT) == epoch;
            if (__all(ok)) break;
            __builtin_amdgcn_s_sleep(1);
            if (__builtin_amdgcn_s_memrealtime() - t0 > 2000000ull) break;
        }
        if (lane == 0) __hip_atomic_store((GAS u64*)(ws + WS_G), epoch, RLX_AGENT);
    }
    STAMP(19);
}

extern "C" void kernel_launch(void* const* d_in, const int* in_sizes, int n_in, void* d_out, int out_size, void* d_ws, size_t ws_size,
                              hipStream_t stream) {
    (void)hipFuncSetAttribute((const void*)k_fused, hipFuncAttributeMaxDynamicSharedMemorySize, LDS_BYTES);
    k_fused<<<256, 512, LDS_BYTES, stream>>>((const char*)d_in[0], (const char*)d_in[1], (const char*)d_in[2], (const char*)d_in[3],
                                             (const char*)d_in[4], (const char*)d_in[5], (char*)d_out, (char*)d_ws);
}
```

```cpp
#include <hip/hip_runtime.h>
#include <hip/hip_bf16.h>
#include <stdint.h>
#include <type_traits>

typedef __bf16 bf16x8 __attribute__((ext_vector_type(8)));
typedef __bf16 bf16x2 __attribute__((ext_vector_type(2)));
typedef float f32x16 __attribute__((ext_vector_type(16)));
typedef float f32x2 __attribute__((ext_vector_type(2)));
typedef float f32x4 __attribute__((ext_vector_type(4)));
typedef short s16x4 __attribute__((ext_vector_type(4)));
typedef short s16x8 __attribute__((ext_vector_type(8)));
typedef unsigned u32x4 __attribute__((ext_vector_type(4)));
typedef unsigned long long u64;
#define LAS __attribute__((address_space(3)))
#define GAS __attribute__((address_space(1)))
#define DI __device__ __forceinline__
#define MFMA(a, b, c) __builtin_amdgcn_mfma_f32_32x32x16_bf16((a), (b), (c), 0, 0, 0)
#define RLX_AGENT __ATOMIC_RELAXED, __HIP_MEMORY_SCOPE_AGENT

constexpr int S_ = 2048, D_ = 448, NH = 16, DH = 28;
constexpr int XROW = D_ * 2;
constexpr int WROW = 3 * D_ * 2;
constexpr int OROW = 2 * D_ * 2;
constexpr int NTILE = S_ / 32;
constexpr unsigned WS_G = 0;
constexpr unsigned WS_FK = 4096;
constexpr unsigned WS_FV = WS_FK + 256 * 128;
constexpr unsigned WS_FO = WS_FV + 256 * 128;
constexpr unsigned WS_K = 1u << 20;
constexpr unsigned KARR_BYTES = 2u * NH * NTILE * 2048;
constexpr unsigned WS_V = WS_K + KARR_BYTES;
constexpr unsigned WS_O = WS_V + KARR_BYTES;
constexpr unsigned WS_END = WS_O + KARR_BYTES;
constexpr float SCALE_LOG2E = 0.18898223650461362f * 1.4426950408889634f;
constexpr int AUX_SC1 = 16;

DI unsigned pk2(float a, float b) {
    f32x2 p = {a, b};
    return __builtin_bit_cast(unsigned, __builtin_convertvector(p, bf16x2));
}
DI u32x4 pack8u(const f32x16& x, int s) {
    u32x4 w = {pk2(x[8 * s + 0], x[8 * s + 1]), pk2(x[8 * s + 2], x[8 * s + 3]), pk2(x[8 * s + 4], x[8 * s + 5]), pk2(x[8 * s + 6], x[8 * s + 7])};
    return w;
}
DI bf16x8 pack8(const f32x16& x, int s) { return __builtin_bit_cast(bf16x8, pack8u(x, s)); }
DI void glds16(unsigned voff, const void* sbase, unsigned lds_base) {
    unsigned sv;
    asm volatile("s_mov_b32 %0, m0\n\ts_mov_b32 m0, %3\n\ts_nop 0\n\tglobal_load_lds_dwordx4 %1, %2\n\ts_mov_b32 m0, %0"
                 : "=&s"(sv) : "v"(voff), "s"(sbase), "s"(lds_base) : "memory");
}
typedef LAS char* lptr;
DI bf16x8 lds_b128(lptr p) { return *(const LAS bf16x8*)p; }
DI void lds_w128(lptr p, u32x4 v) { *(LAS u32x4*)p = v; }
DI s16x4 lds_tr(lptr p) { return __builtin_amdgcn_ds_read_tr16_b64_v4i16((LAS s16x4*)p); }
DI bf16x8 cat8(s16x4 lo, s16x4 hi) { s16x8 v = __builtin_shufflevector(lo, hi, 0, 1, 2, 3, 4, 5, 6, 7); return __builtin_bit_cast(bf16x8, v); }
DI bf16x8 tr_frag(lptr a0, lptr a1) { return cat8(lds_tr(a0), lds_tr(a1)); }
#define SBAR() __builtin_amdgcn_sched_barrier(0)

#define STAMP(i)
#define WAITV(N) asm volatile("s_waitcnt vmcnt(" #N ")" ::: "memory")
#define WAITV_BAR(N) asm volatile("s_waitcnt vmcnt(" #N ") lgkmcnt(0)\n\ts_barrier" ::: "memory")
#define LBAR() asm volatile("s_waitcnt lgkmcnt(0)\n\ts_barrier" ::: "memory")
template <int N> DI void waitv_bar() {
    if constexpr (N == 2) WAITV_BAR(2); else if constexpr (N == 3) WAITV_BAR(3); else if constexpr (N == 4) WAITV_BAR(4);
    else if constexpr (N == 6) WAITV_BAR(6); else if constexpr (N == 12) WAITV_BAR(12); else LBAR();
}

DI void flag_store(__amdgpu_buffer_rsrc_t rs, unsigned off, int lane, u64 e) {
    typedef unsigned u32x2 __attribute__((ext_vector_type(2)));
    const u32x2 v = {(unsigned)e, (unsigned)(e >> 32)};
    __builtin_amdgcn_raw_buffer_store_b64(v, rs, lane == 0 ? off : 0xF0000000u, 0, AUX_SC1);
}
DI void wait_flags(const char* ws, unsigned off, bool active, u64 e) {
    const GAS u64* p = (const GAS u64*)(ws + off);
    const u64 t0 = __builtin_amdgcn_s_memrealtime();
    for (;;) {
        u64 v = e;
        if (active) v = __hip_atomic_load(p, RLX_AGENT);
        if (__all(v == e)) break;
        __builtin_amdgcn_s_sleep(1);
        if (__builtin_amdgcn_s_memrealtime() - t0 > 2000000ull) break;
    }
    asm volatile("" ::: "memory");
}

constexpr int L_X = 0;
constexpr int L_W = 98304;
constexpr int L_V = 0;
constexpr int L_WO0 = 49152;
constexpr int L_WO1 = 106496;
constexpr int L_K = 114688;
constexpr int LDS_BYTES = 163840;
constexpr int wslot(int n) { return (n >= 12 && n < 18) ? 49152 + (n - 12) * 8192 : L_W + (n % 6) * 8192; }
constexpr int npieces(int n) { return n >= 21 ? 0 : n < 7 ? 3 : 1; }

#define ROT 1

__global__ __launch_bounds__(512, 2) void k_fused(const char* __restrict__ xA, const char* __restrict__ wA, const char* __restrict__ woA,
                                                  const char* __restrict__ xB, const char* __restrict__ wB, const char* __restrict__ woB,
                                                  char* __restrict__ out, char* __restrict__ ws) {
    extern __shared__ __attribute__((aligned(16))) char lds[];
    const u64 epoch = *(const u64*)(ws + WS_G) + 1ull;
    STAMP(0);
    const int xcd = blockIdx.x & 7, idx = blockIdx.x >> 3;
    const int b = xcd >> 2;
    const int r = ((xcd >> 1) & 1) * 8 + (idx >> 2);
    const int g = (xcd & 1) * 4 + (idx & 3);
    const int wg = (b * 16 + r) * 8 + g;
    const int tid = threadIdx.x, lane = tid & 63;
    const int w = __builtin_amdgcn_readfirstlane(tid >> 6);
    const int hh = w >> 2, t = w & 3;
    const char* xb = (b ? xB : xA) + (size_t)r * 128 * XROW;
    const char* wb = (b ? wB : wA) + g * 112;
    const unsigned lbase = (unsigned)(size_t)lds;
    const lptr L = (lptr)lds;
    const __amdgpu_buffer_rsrc_t rs = __builtin_amdgcn_make_buffer_rsrc(ws, 0, WS_END, 0x00020000);

    const unsigned xo0 = (unsigned)((16 * w + (lane >> 3)) * XROW + (((lane & 7) ^ (lane >> 4)) << 4));
    const unsigned xo1 = (unsigned)((16 * w + 8 + (lane >> 3)) * XROW + (((lane & 7) ^ (4 + (lane >> 4))) << 4));
    int wc = (lane & 7) ^ (((lane >> 4) & 1) << 2); wc = wc > 6 ? 6 : wc;
    const unsigned wo0 = (unsigned)((8 * w + (lane >> 3)) * WROW + wc * 16);
    const int rot = ROT ? (2 * (r & 7) + 3 * (g & 3)) % 7 : 0;

    auto issue_piece = [&](int n, int k) {
        const int pass = n / 7, tau = n % 7;
        int s = tau + rot; s = s >= 7 ? s - 7 : s;
        const unsigned poff = pass == 0 ? 896u : pass == 1 ? 1792u : 0u;
        if (k == 0) glds16(wo0 + s * 64 * WROW + poff, wb, lbase + wslot(n) + w * 1024);
        else glds16((k == 1 ? xo0 : xo1) + s * 128, xb, lbase + L_X + (n % 6) * 16384 + w * 2048 + (k - 1) * 1024);
    };
    auto issue_item = [&](int it) {
#pragma unroll
        for (int k = 0; k < npieces(2 * it); ++k) issue_piece(2 * it, k);
#pragma unroll
        for (int k = 0; k < npieces(2 * it + 1); ++k) issue_piece(2 * it + 1, k);
    };
    auto issue_nth = [&](int it, int p) {
        const int n0 = it <= 2 ? 2 * (it + 3) : it == 3 ? 12 : it <= 6 ? 2 * (it + 4) : 21;
        const int n1 = it <= 2 ? n0 + 2 : it == 3 ? 16 : it <= 6 ? n0 + 2 : 21;
        int c = 0;
#pragma unroll
        for (int n = n0; n < n1; ++n)
#pragma unroll
            for (int k = 0; k < npieces(n); ++k) { if (c == p) issue_piece(n, k); ++c; }
    };
    issue_item(0); issue_item(1); issue_item(2);

    const int r31 = lane & 31, h = lane >> 5;
    const int xrow = 32 * t + r31;
    const unsigned xa = (unsigned)(xrow * 128 + ((h ^ ((xrow >> 1) & 7)) << 4));
    lptr xp0 = L + L_X + xa, xp1 = L + L_X + (xa ^ 32), xp2 = L + L_X + (xa ^ 64), xp3 = L + L_X + (xa ^ 96);
    lptr xq0 = xp0 + 49152, xq1 = xp1 + 49152, xq2 = xp2 + 49152, xq3 = xp3 + 49152;
    asm volatile("" : "+v"(xq0), "+v"(xq1), "+v"(xq2), "+v"(xq3));
    const int blk = (lane >> 4) & 1, q = (lane & 15) >> 2, pp = lane & 3;
    const unsigned wsw = ((q >> 1) & 1) << 6;
    lptr waX = L + 49152 + (unsigned)((8 * h + q) * 128 + ((56 * hh + 32 * blk + 8 * pp) ^ wsw));
    lptr waR = waX + (L_W - 49152);
    asm volatile("" : "+v"(waX), "+v"(waR));

    bf16x8 xf[28];
    f32x16 accK, accV, accQ;
    bf16x8 q0, q1;
    const unsigned gko = (unsigned)(((b * NH + 2 * g + hh) * NTILE + 4 * r + t) * 2048) + lane * 16;
    const lptr ownK = L + L_K + ((8 + t) * 2 + hh) * 2048 + lane * 16, ownV = L + L_V + ((8 + t) * 2 + hh) * 2048 + lane * 16;
    const int j0 = (2 * w) & 7;
    const bool hv0 = r - 2 + (j0 >> 2) >= 0;
    const unsigned hgo = (unsigned)(((b * NH + 2 * g + hh) * NTILE + (hv0 ? 4 * r - 8 + j0 : 0)) * 2048) + lane * 16;
    const unsigned hfo = (unsigned)((hv0 ? wg - 8 * (2 - (j0 >> 2)) : wg) * 128 + (hh * 4 + ((j0 + (lane & 1)) & 3)) * 8);
    u32x4 hk[4];
    u64 kflag = 0;

    auto ldk = [&](int n, int ks, bf16x8& wf) {
        const int pass = n / 7, tau = n % 7;
        const lptr wbase = (wslot(n) >= L_W ? waR + (wslot(n) - L_W) : waX + (wslot(n) - 49152)) + ks * 2048;
        if (pass == 0) {
            if (n % 6 < 3) xf[4 * tau + ks] = lds_b128((ks == 0 ? xp0 : ks == 1 ? xp1 : ks == 2 ? xp2 : xp3) + (n % 6) * 16384);
            else xf[4 * tau + ks] = lds_b128((ks == 0 ? xq0 : ks == 1 ? xq1 : ks == 2 ? xq2 : xq3) + (n % 6 - 3) * 16384);
        }
        wf = tr_frag(wbase, wbase + 512);
    };
    auto mma = [&](int n, int ks, const bf16x8& wf) {
        const int pass = n / 7, tau = n % 7;
        const bool first = tau == 0 && ks == 0;
        const f32x16 zero = {};
        if (pass == 0) accK = MFMA(wf, xf[4 * tau + ks], first ? zero : accK);
        else if (pass == 1) accV = MFMA(xf[4 * tau + ks], wf, first ? zero : accV);
        else accQ = MFMA(wf, xf[4 * tau + ks], first ? zero : accQ);
    };
    u32x4 e0, e1;
    auto hook = [&](int it, int k) {
        if (it == 3 && k == 5) {
            e0 = pack8u(accK, 0); e1 = pack8u(accK, 1);
            e1[2] = h ? 0u : e1[2]; e1[3] = h ? 0u : e1[3];
        }
        if (it == 3 && k == 6) { lds_w128(ownK, e0); lds_w128(ownK + 1024, e1); }
        if (it == 3 && k == 7) {
            __builtin_amdgcn_raw_buffer_store_b128(e0, rs, WS_K + gko, 0, AUX_SC1);
            __builtin_amdgcn_raw_buffer_store_b128(e1, rs, WS_K + gko + 1024, 0, AUX_SC1);
        }
        if (it == 7 && k == 0)
            flag_store(rs, WS_FK + wg * 128 + w * 8, lane, epoch);
        if (it == 7 && k == 1) {
            e0 = pack8u(accV, 0); e1 = pack8u(accV, 1);
            const unsigned vv = (r31 == DH) ? 0x3F803F80u : 0u;
#pragma unroll
            for (int i = 0; i < 4; ++i) { e0[i] = r31 >= DH ? vv : e0[i]; e1[i] = r31 >= DH ? vv : e1[i]; }
        }
        if (it == 7 && k == 2) { lds_w128(ownV, e0); lds_w128(ownV + 1024, e1); }
        if (it == 7 && k == 3) {
            __builtin_amdgcn_raw_buffer_store_b128(e0, rs, WS_V + gko, 0, AUX_SC1);
            __builtin_amdgcn_raw_buffer_store_b128(e1, rs, WS_V + gko + 1024, 0, AUX_SC1);
        }
        if (it == 9 && k == 0) kflag = __hip_atomic_load((const GAS u64*)(ws + WS_FK + hfo), RLX_AGENT);
    };

#define FS(i)
    bf16x8 wf[2][8];
    waitv_bar<12>();
#pragma unroll
    for (int k = 0; k < 8; ++k) ldk(k >> 2, k & 3, wf[0][k]);
    auto run_item = [&](auto IT) {
        constexpr int it = decltype(IT)::value;
        constexpr int cnt = it == 0 ? 6 : it == 1 ? 4 : it <= 3 ? 2 : it <= 6 ? 6 : it == 7 ? 3 : it == 8 ? 4 : it == 9 ? 3 : -1;
        waitv_bar<cnt>();
        FS(it);
        if (it == 10) {
            if (hv0) {
                if (!__all(lane >= 2 || kflag == epoch)) wait_flags(ws, WS_FK + hfo, lane < 2, epoch);
                asm volatile("" ::: "memory");
#pragma unroll
                for (int k = 0; k < 4; ++k) hk[k] = __builtin_amdgcn_raw_buffer_load_b128(rs, WS_K + hgo + k * 1024, 0, AUX_SC1);
            } else {
#pragma unroll
                for (int k = 0; k < 4; ++k) hk[k] = (u32x4){0u, 0u, 0u, 0u};
            }
        }
        SBAR();
#pragma unroll
        for (int k = 0; k < 8; ++k) {
            const int n = 2 * it + (k >> 2);
            if (n < 21) mma(n, k & 3, wf[it & 1][k]);
            if (2 * it + 2 + (k >> 2) < 21) ldk(2 * it + 2 + (k >> 2), k & 3, wf[(it + 1) & 1][k]);
            {
                constexpr int np = it == 0 ? 4 : it <= 2 ? 2 : it == 3 ? 4 : it <= 5 ? 2 : it == 6 ? 1 : 0;
                if (np == 4) { if ((k & 1) == 0 && !(it == 3)) issue_nth(it, k >> 1); if (it == 3 && k < 4) issue_nth(it, k); }
                else if (np == 2) { if ((k & 3) == 1) issue_nth(it, k >> 2); }
                else if (np == 1) { if (k == 1) issue_nth(it, 0); }
            }
            hook(it, k);
            SBAR();
        }
    };
    run_item(std::integral_constant<int, 0>{}); run_item(std::integral_constant<int, 1>{}); run_item(std::integral_constant<int, 2>{});
    run_item(std::integral_constant<int, 3>{}); run_item(std::integral_constant<int, 4>{}); run_item(std::integral_constant<int, 5>{});
    run_item(std::integral_constant<int, 6>{}); run_item(std::integral_constant<int, 7>{}); run_item(std::integral_constant<int, 8>{});
    run_item(std::integral_constant<int, 9>{}); run_item(std::integral_constant<int, 10>{});
    q0 = pack8(accQ, 0);
    { u32x4 e = pack8u(accQ, 1); e[2] = h ? 0u : e[2]; e[3] = h ? 0u : e[3]; q1 = __builtin_bit_cast(bf16x8, e); }
    FS(11);
    STAMP(12);

    if (hv0) WAITV(4); else WAITV(0);
    flag_store(rs, WS_FV + wg * 128 + w * 8, lane, epoch);
    LBAR();
    STAMP(13);
    const int ct = g & 3, rb3 = 2 * r + (g >> 2);
    const char* wob = (b ? woB : woA) + ct * 256;
    const unsigned wo3 = (unsigned)((w * 56 + (lane >> 3)) * XROW + (((lane & 7) ^ (((lane >> 4) & 1) << 2)) << 4));
    {
        const lptr lo = L + L_K + (j0 * 2 + hh) * 2048 + lane * 16;
        lds_w128(lo, hk[0]); lds_w128(lo + 1024, hk[1]); lds_w128(lo + 4096, hk[2]); lds_w128(lo + 4096 + 1024, hk[3]);
    }
    LBAR();
    STAMP(14);
#define WO0(i0, i1) do { _Pragma("unroll") for (int i_ = (i0); i_ < (i1); ++i_) glds16(wo3 + i_ * 8 * XROW, wob, lbase + L_WO0 + (w * 7 + i_) * 1024); } while (0)
#define WO1(i0, i1) do { if (ct != 3) { _Pragma("unroll") for (int i_ = (i0); i_ < (i1); ++i_) glds16(wo3 + 128 + i_ * 8 * XROW, wob, lbase + L_WO1 + (w * 7 + i_) * 1024); } } while (0)
    const float NEG = -__builtin_inff();
    const lptr kb = L + L_K + (t * 2 + hh) * 2048 + lane * 16;
    const lptr vb = L + L_V + (t * 2 + hh) * 2048 + lane * 16;
    f32x16 o = {};
    {
        f32x16 sc[9];
        u64 vflag = __hip_atomic_load((const GAS u64*)(ws + WS_FV + hfo), RLX_AGENT);
#pragma unroll
        for (int j = 0; j < 9; ++j) {
            const bf16x8 k0 = lds_b128(kb + j * 4096), k1 = lds_b128(kb + j * 4096 + 1024);
            f32x16 z = {};
            z = MFMA(k0, q0, z);
            sc[j] = MFMA(k1, q1, z);
        }
        WO0(0, 4);
        if (__builtin_expect(r < 2, 0)) {
            asm volatile("" ::: "memory");
#pragma unroll
            for (int j = 0; j < 8; ++j)
                if (4 * r - 8 + t + j < 0) {
                    asm volatile("" ::: "memory");
#pragma unroll
                    for (int i = 0; i < 16; ++i) sc[j][i] = NEG;
                }
        }
#pragma unroll
        for (int i = 0; i < 16; ++i) {
            const int c = (i & 3) + 8 * (i >> 2) + 4 * h;
            sc[8][i] = c > r31 ? sc[0][i] : sc[8][i];
        }
        float m = NEG;
#pragma unroll
        for (int j = 1; j < 9; ++j)
#pragma unroll
            for (int i = 0; i < 16; ++i) m = __builtin_fmaxf(m, sc[j][i]);
        {
            auto rr = __builtin_amdgcn_permlane32_swap(__float_as_uint(m), __float_as_uint(m), false, false);
            m = __builtin_fmaxf(__uint_as_float(rr[0]), __uint_as_float(rr[1]));
        }
        WO0(4, 7);
        u32x4 hv[4];
        if (hv0) {
            if (!__all(lane >= 2 || vflag == epoch)) wait_flags(ws, WS_FV + hfo, lane < 2, epoch);
            asm volatile("" ::: "memory");
#pragma unroll
            for (int k = 0; k < 4; ++k) hv[k] = __builtin_amdgcn_raw_buffer_load_b128(rs, WS_V + hgo + k * 1024, 0, AUX_SC1);
        } else {
#pragma unroll
            for (int k = 0; k < 4; ++k) hv[k] = (u32x4){0u, 0u, 0u, 0u};
        }
        STAMP(15);
        const float nm = -m * SCALE_LOG2E;
        bf16x8 pa[9], pb[9];
#define EXP_TILE(j) do { _Pragma("unroll") for (int i_ = 0; i_ < 16; ++i_) sc[j][i_] = __builtin_amdgcn_exp2f(__builtin_fmaf(sc[j][i_], SCALE_LOG2E, nm)); pa[j] = pack8(sc[j], 0); pb[j] = pack8(sc[j], 1); } while (0)
#define PV_TILE(j) do { const bf16x8 v0_ = lds_b128(vb + (j) * 4096), v1_ = lds_b128(vb + (j) * 4096 + 1024); o = MFMA(v0_, pa[j], o); o = MFMA(v1_, pb[j], o); } while (0)
        {
#pragma unroll
            for (int i = 0; i < 16; ++i) sc[8][i] = __builtin_amdgcn_exp2f(__builtin_fmaf(sc[8][i], SCALE_LOG2E, nm));
#pragma unroll
            for (int i = 0; i < 16; ++i) {
                const int c = (i & 3) + 8 * (i >> 2) + 4 * h;
                sc[0][i] = c > r31 ? sc[8][i] : 0.f;
                sc[8][i] = c > r31 ? 0.f : sc[8][i];
            }
            pa[8] = pack8(sc[8], 0); pb[8] = pack8(sc[8], 1); pa[0] = pack8(sc[0], 0); pb[0] = pack8(sc[0], 1);
        }
        PV_TILE(8);
        EXP_TILE(7); EXP_TILE(6); EXP_TILE(5); EXP_TILE(4);
        {
            const lptr lo = L + L_V + (j0 * 2 + hh) * 2048 + lane * 16;
            lds_w128(lo, hv[0]); lds_w128(lo + 1024, hv[1]); lds_w128(lo + 4096, hv[2]); lds_w128(lo + 4096 + 1024, hv[3]);
        }
        LBAR();
        STAMP(16);
        WO1(0, 2); PV_TILE(7); EXP_TILE(3); WO1(2, 4); PV_TILE(6); EXP_TILE(2); WO1(4, 6); PV_TILE(5); EXP_TILE(1); WO1(6, 7); PV_TILE(4); PV_TILE(0);
        PV_TILE(3); PV_TILE(2); PV_TILE(1);
    }
    STAMP(17);
    {
        float l = o[12];
        auto rr = __builtin_amdgcn_permlane32_swap(__float_as_uint(l), __float_as_uint(l), false, false);
        l = __uint_as_float(rr[1]);
        const float inv = 1.0f / l;
#pragma unroll
        for (int i = 0; i < 16; ++i) o[i] *= inv;
        if (h == 1) {
#pragma unroll
            for (int i = 12; i < 16; ++i) o[i] = 0.f;
        }
        const unsigned go = WS_O + (unsigned)(((b * NTILE + 4 * r + t) * NH + 2 * g + hh) * 2048) + lane * 16;
        __builtin_amdgcn_raw_buffer_store_b128(pack8u(o, 0), rs, go, 0, AUX_SC1);
        __builtin_amdgcn_raw_buffer_store_b128(pack8u(o, 1), rs, go + 1024, 0, AUX_SC1);
        WAITV(0);
        flag_store(rs, WS_FO + wg * 128 + w * 8, lane, epoch);
    }
    STAMP(18);

    const int tt = w & 1, kq = w >> 1;
    const int nimg = ct == 3 ? 1 : 2;
    {
        const int ts = 2 * (g >> 2) + tt;
        wait_flags(ws, WS_FO + (unsigned)(((b * 16 + r) * 8 + 2 * kq + ((lane >> 1) & 1)) * 128 + ((lane & 1) * 4 + ts) * 8), lane < 4, epoch);
    }
    STAMP(19);
    bf16x8 of[8];
    {
        const unsigned go = WS_O + (unsigned)(((b * NTILE + 2 * rb3 + tt) * NH + 4 * kq) * 2048) + lane * 16;
#pragma unroll
        for (int i = 0; i < 8; ++i) of[i] = __builtin_bit_cast(bf16x8, __builtin_amdgcn_raw_buffer_load_b128(rs, go + i * 1024, 0, AUX_SC1));
    }
    WAITV_BAR(8);
    STAMP(20);
    f32x16 acc3[4] = {};
    {
        const int row0 = 4 * kq * DH + 4 * h + q;
        int rl = row0 + 3 * DH + 16 + 8; rl = rl > 447 ? (rl & 3) + 444 : rl;
        lptr pn[4], pl[4];
#pragma unroll
        for (int i = 0; i < 4; ++i) {
            const int n = (kq + i) & 3;
            const unsigned co = (n >> 1) * (L_WO1 - L_WO0) + ((((n & 1) << 6) + 32 * blk + 8 * pp) ^ wsw);
            pn[i] = L + L_WO0 + row0 * 128 + co; pl[i] = L + L_WO0 + rl * 128 + co;
        }
#pragma unroll
        for (int i = 0; i < 4; ++i)
#pragma unroll
            for (int s = 0; s < 2; ++s) {
                const int ro = (i * DH + 16 * s) * 128;
                const bool last = i == 3 && s == 1;
#pragma unroll
                for (int n = 0; n < 4; ++n) acc3[n] = MFMA(tr_frag(pn[n] + ro, last ? pl[n] : pn[n] + ro + 1024), of[2 * i + s], acc3[n]);
            }
    }
    STAMP(21);
    LBAR();
    {
#pragma unroll
        for (int i = 1; i < 4; ++i) {
            const lptr dst = L + tt * 49152 + ((kq + i) & 3) * 12288 + (i - 1) * 4096 + lane * 16;
#pragma unroll
            for (int e = 0; e < 4; ++e) {
                const f32x4 v = {acc3[i][4 * e], acc3[i][4 * e + 1], acc3[i][4 * e + 2], acc3[i][4 * e + 3]};
                *(LAS f32x4*)(dst + e * 1024) = v;
            }
        }
    }
    LBAR();
    f32x16 fin = acc3[0];
    {
        const lptr xr = L + tt * 49152 + kq * 12288 + lane * 16;
#pragma unroll
        for (int k = 0; k < 3; ++k)
#pragma unroll
            for (int e = 0; e < 4; ++e) {
                const f32x4 v = *(const LAS f32x4*)(xr + k * 4096 + e * 1024);
                fin[4 * e] += v[0]; fin[4 * e + 1] += v[1]; fin[4 * e + 2] += v[2]; fin[4 * e + 3] += v[3];
            }
    }
    if (kq < 2 * nimg) {
        char* orow = out + (size_t)(64 * rb3 + 32 * tt + r31) * OROW + (b * D_ + 128 * ct + 32 * kq) * 2;
#pragma unroll
        for (int qq = 0; qq < 4; qq += 2) {
            unsigned ax = pk2(fin[4 * qq], fin[4 * qq + 1]), ay = pk2(fin[4 * qq + 2], fin[4 * qq + 3]);
            unsigned bx = pk2(fin[4 * qq + 4], fin[4 * qq + 5]), by = pk2(fin[4 * qq + 6], fin[4 * qq + 7]);
            auto r0 = __builtin_amdgcn_permlane32_swap(ax, bx, false, false);
            auto r1 = __builtin_amdgcn_permlane32_swap(ay, by, false, false);
            u32x4 v = {r0[0], r1[0], r0[1], r1[1]};
            *(u32x4*)(orow + (8 * qq + 8 * h) * 2) = v;
        }
    }
    STAMP(22);
    if (wg == 0 && w == 0) {
        const u64 t0 = __builtin_amdgcn_s_memrealtime();
        for (;;) {
            bool ok = true;
#pragma unroll
            for (int k = 0; k < 4; ++k) ok &= __hip_atomic_load((const GAS u64*)(ws + WS_FO + (k * 64 + lane) * 128), RLX_AGENT) == epoch;
            if (__all(ok)) break;
            __builtin_amdgcn_s_sleep(1);
            if (__builtin_amdgcn_s_memrealtime() - t0 > 2000000ull) break;
        }
        if (lane == 0) __hip_atomic_store((GAS u64*)(ws + WS_G), epoch, RLX_AGENT);
    }
    STAMP(23);
}

extern "C" void kernel_launch(void* const* d_in, const int* in_sizes, int n_in, void* d_out, int out_size, void* d_ws, size_t ws_size,
                              hipStream_t stream) {
    (void)hipFuncSetAttribute((const void*)k_fused, hipFuncAttributeMaxDynamicSharedMemorySize, LDS_BYTES);
    k_fused<<<256, 512, LDS_BYTES, stream>>>((const char*)d_in[0], (const char*)d_in[1], (const char*)d_in[2], (const char*)d_in[3],
                                             (const char*)d_in[4], (const char*)d_in[5], (char*)d_out, (char*)d_ws);
}
```

```cpp
#include <hip/hip_runtime.h>
#include <hip/hip_bf16.h>
#include <stdint.h>
#include <type_traits>

typedef __bf16 bf16x8 __attribute__((ext_vector_type(8)));
typedef __bf16 bf16x2 __attribute__((ext_vector_type(2)));
typedef float f32x16 __attribute__((ext_vector_type(16)));
typedef float f32x2 __attribute__((ext_vector_type(2)));
typedef float f32x4 __attribute__((ext_vector_type(4)));
typedef short s16x4 __attribute__((ext_vector_type(4)));
typedef short s16x8 __attribute__((ext_vector_type(8)));
typedef unsigned u32x4 __attribute__((ext_vector_type(4)));
typedef unsigned long long u64;
#define LAS __attribute__((address_space(3)))
#define GAS __attribute__((address_space(1)))
#define DI __device__ __forceinline__
#define MFMA(a, b, c) __builtin_amdgcn_mfma_f32_32x32x16_bf16((a), (b), (c), 0, 0, 0)
#define RLX_AGENT __ATOMIC_RELAXED, __HIP_MEMORY_SCOPE_AGENT

constexpr int S_ = 2048, D_ = 448, NH = 16, DH = 28;
constexpr int XROW = D_ * 2;
constexpr int WROW = 3 * D_ * 2;
constexpr int OROW = 2 * D_ * 2;
constexpr int NTILE = S_ / 32;
constexpr unsigned WS_G = 0;
constexpr unsigned WS_FK = 4096;
constexpr unsigned WS_FV = WS_FK + 256 * 128;
constexpr unsigned WS_FO = WS_FV + 256 * 128;
constexpr unsigned WS_K = 1u << 20;
constexpr unsigned KARR_BYTES = 2u * NH * NTILE * 2048;
constexpr unsigned WS_V = WS_K + KARR_BYTES;
constexpr unsigned WS_O = WS_V + KARR_BYTES;
constexpr unsigned WS_END = WS_O + KARR_BYTES;
constexpr float SCALE_LOG2E = 0.18898223650461362f * 1.4426950408889634f;
constexpr int AUX_SC1 = 16;

DI unsigned pk2(float a, float b) {
    f32x2 p = {a, b};
    return __builtin_bit_cast(unsigned, __builtin_convertvector(p, bf16x2));
}
DI u32x4 pack8u(const f32x16& x, int s) {
    u32x4 w = {pk2(x[8 * s + 0], x[8 * s + 1]), pk2(x[8 * s + 2], x[8 * s + 3]), pk2(x[8 * s + 4], x[8 * s + 5]), pk2(x[8 * s + 6], x[8 * s + 7])};
    return w;
}
DI bf16x8 pack8(const f32x16& x, int s) { return __builtin_bit_cast(bf16x8, pack8u(x, s)); }
DI void glds16(unsigned voff, const void* sbase, unsigned lds_base) {
    unsigned sv;
    asm volatile("s_mov_b32 %0, m0\n\ts_mov_b32 m0, %3\n\ts_nop 0\n\tglobal_load_lds_dwordx4 %1, %2\n\ts_mov_b32 m0, %0"
                 : "=&s"(sv) : "v"(voff), "s"(sbase), "s"(lds_base) : "memory");
}
typedef LAS char* lptr;
DI bf16x8 lds_b128(lptr p) { return *(const LAS bf16x8*)p; }
DI void lds_w128(lptr p, u32x4 v) { *(LAS u32x4*)p = v; }
DI s16x4 lds_tr(lptr p) { return __builtin_amdgcn_ds_read_tr16_b64_v4i16((LAS s16x4*)p); }
DI bf16x8 cat8(s16x4 lo, s16x4 hi) { s16x8 v = __builtin_shufflevector(lo, hi, 0, 1, 2, 3, 4, 5, 6, 7); return __builtin_bit_cast(bf16x8, v); }
DI bf16x8 tr_frag(lptr a0, lptr a1) { return cat8(lds_tr(a0), lds_tr(a1)); }
#define SBAR() __builtin_amdgcn_sched_barrier(0)

#define STAMP(i)
#define WAITV(N) asm volatile("s_waitcnt vmcnt(" #N ")" ::: "memory")
#define WAITV_BAR(N) asm volatile("s_waitcnt vmcnt(" #N ") lgkmcnt(0)\n\ts_barrier" ::: "memory")
#define LBAR() asm volatile("s_waitcnt lgkmcnt(0)\n\ts_barrier" ::: "memory")
template <int N> DI void waitv_bar() {
    if constexpr (N == 2) WAITV_BAR(2); else if constexpr (N == 3) WAITV_BAR(3); else if constexpr (N == 4) WAITV_BAR(4);
    else if constexpr (N == 6) WAITV_BAR(6); else if constexpr (N == 12) WAITV_BAR(12); else LBAR();
}

DI void flag_store(__amdgpu_buffer_rsrc_t rs, unsigned off, int lane, u64 e) {
    typedef unsigned u32x2 __attribute__((ext_vector_type(2)));
    const u32x2 v = {(unsigned)e, (unsigned)(e >> 32)};
    __builtin_amdgcn_raw_buffer_store_b64(v, rs, lane == 0 ? off : 0xF0000000u, 0, AUX_SC1);
}
DI void wait_flags(const char* ws, unsigned off, bool active, u64 e) {
    const GAS u64* p = (const GAS u64*)(ws + off);
    const u64 t0 = __builtin_amdgcn_s_memrealtime();
    for (;;) {
        u64 v = e;
        if (active) v = __hip_atomic_load(p, RLX_AGENT);
        if (__all(v == e)) break;
        __builtin_amdgcn_s_sleep(1);
        if (__builtin_amdgcn_s_memrealtime() - t0 > 2000000ull) break;
    }
    asm volatile("" ::: "memory");
}

constexpr int L_X = 0;
constexpr int L_W = 98304;
constexpr int L_V = 0;
constexpr int L_WO0 = 49152;
constexpr int L_WO1 = 106496;
constexpr int L_K = 114688;
constexpr int LDS_BYTES = 163840;
constexpr int wslot(int n) { return (n >= 12 && n < 18) ? 49152 + (n - 12) * 8192 : L_W + (n % 6) * 8192; }
constexpr int npieces(int n) { return n >= 21 ? 0 : n < 7 ? 3 : 1; }

#define PRETOUCH 1
#define ROT 1

__global__ __launch_bounds__(512, 2) void k_fused(const char* __restrict__ xA, const char* __restrict__ wA, const char* __restrict__ woA,
                                                  const char* __restrict__ xB, const char* __restrict__ wB, const char* __restrict__ woB,
                                                  char* __restrict__ out, char* __restrict__ ws) {
    extern __shared__ __attribute__((aligned(16))) char lds[];
    const u64 epoch = *(const u64*)(ws + WS_G) + 1ull;
    STAMP(0);
    const int xcd = blockIdx.x & 7, idx = blockIdx.x >> 3;
    const int b = xcd >> 2;
    const int r = ((xcd >> 1) & 1) * 8 + (idx >> 2);
    const int g = (xcd & 1) * 4 + (idx & 3);
    const int wg = (b * 16 + r) * 8 + g;
    const int tid = threadIdx.x, lane = tid & 63;
    const int w = __builtin_amdgcn_readfirstlane(tid >> 6);
    const int hh = w >> 2, t = w & 3;
    const char* xb = (b ? xB : xA) + (size_t)r * 128 * XROW;
    const char* wb = (b ? wB : wA) + g * 112;
    const unsigned lbase = (unsigned)(size_t)lds;
    const lptr L = (lptr)lds;
    const __amdgpu_buffer_rsrc_t rs = __builtin_amdgcn_make_buffer_rsrc(ws, 0, WS_END, 0x00020000);

    const unsigned xo0 = (unsigned)((16 * w + (lane >> 3)) * XROW + (((lane & 7) ^ (lane >> 4)) << 4));
    const unsigned xo1 = (unsigned)((16 * w + 8 + (lane >> 3)) * XROW + (((lane & 7) ^ (4 + (lane >> 4))) << 4));
    int wc = (lane & 7) ^ (((lane >> 4) & 1) << 2); wc = wc > 6 ? 6 : wc;
    const unsigned wo0 = (unsigned)((8 * w + (lane >> 3)) * WROW + wc * 16);
    const int rot = ROT ? (2 * (r & 7) + 3 * (g & 3)) % 7 : 0;

    auto issue_piece = [&](int n, int k) {
        const int pass = n / 7, tau = n % 7;
        int s = tau + rot; s = s >= 7 ? s - 7 : s;
        const unsigned poff = pass == 0 ? 896u : pass == 1 ? 1792u : 0u;
        if (k == 0) glds16(wo0 + s * 64 * WROW + poff, wb, lbase + wslot(n) + w * 1024);
        else glds16((k == 1 ? xo0 : xo1) + s * 128, xb, lbase + L_X + (n % 6) * 16384 + w * 2048 + (k - 1) * 1024);
    };
    auto issue_item = [&](int it) {
#pragma unroll
        for (int k = 0; k < npieces(2 * it); ++k) issue_piece(2 * it, k);
#pragma unroll
        for (int k = 0; k < npieces(2 * it + 1); ++k) issue_piece(2 * it + 1, k);
    };
    auto issue_nth = [&](int it, int p) {
        const int n0 = it <= 2 ? 2 * (it + 3) : it == 3 ? 12 : it <= 6 ? 2 * (it + 4) : 21;
        const int n1 = it <= 2 ? n0 + 2 : it == 3 ? 16 : it <= 6 ? n0 + 2 : 21;
        int c = 0;
#pragma unroll
        for (int n = n0; n < n1; ++n)
#pragma unroll
            for (int k = 0; k < npieces(n); ++k) { if (c == p) issue_piece(n, k); ++c; }
    };
    unsigned dummy = 0;
    if (w == 7) {
        const int ct_ = g & 3, rb3_ = 2 * r + (g >> 2);
        const char* p = ws + WS_FK + wg * 128;
        if (lane == 1) p = ws + WS_FV + wg * 128;
        if (lane == 2) p = ws + WS_FO + wg * 128;
        if (lane == 3) p = ws + WS_K + (unsigned)(((b * NH + 2 * g) * NTILE + 4 * r) * 2048);
        if (lane == 4) p = ws + WS_K + (unsigned)(((b * NH + 2 * g + 1) * NTILE + 4 * r) * 2048);
        if (lane == 5) p = ws + WS_V + (unsigned)(((b * NH + 2 * g) * NTILE + 4 * r) * 2048);
        if (lane == 6) p = ws + WS_V + (unsigned)(((b * NH + 2 * g + 1) * NTILE + 4 * r) * 2048);
        if (lane == 7) p = ws + WS_O + (unsigned)(((b * NTILE + 4 * r) * NH + 2 * g) * 2048);
        if (lane == 8) p = ws + WS_O + (unsigned)(((b * NTILE + 2 * rb3_) * NH) * 2048);
        if (lane == 9) p = out + (size_t)(64 * rb3_) * OROW + (b * D_ + 128 * ct_) * 2;
        if (lane == 10) p = out + (size_t)(64 * rb3_ + 32) * OROW + (b * D_ + 128 * ct_) * 2;
        if (lane == 11) p = (b ? woB : woA) + ct_ * 256;
        if (lane == 12) p = (b ? woB : woA) + ct_ * 256 + 224 * XROW;
        if (lane == 13) p = (b ? woB : woA) + ct_ * 256 + 440 * XROW;
        if (lane == 14 && r >= 2) p = ws + WS_K + (unsigned)(((b * NH + 2 * g) * NTILE + 4 * r - 8) * 2048);
        if (lane == 15 && r >= 2) p = ws + WS_V + (unsigned)(((b * NH + 2 * g) * NTILE + 4 * r - 8) * 2048);
        asm volatile("global_load_dword %0, %1, off" : "+v"(dummy) : "v"(lane < 16 ? p : ws) : "memory");
    }
    issue_item(0); issue_item(1); issue_item(2);

    const int r31 = lane & 31, h = lane >> 5;
    const int xrow = 32 * t + r31;
    const unsigned xa = (unsigned)(xrow * 128 + ((h ^ ((xrow >> 1) & 7)) << 4));
    lptr xp0 = L + L_X + xa, xp1 = L + L_X + (xa ^ 32), xp2 = L + L_X + (xa ^ 64), xp3 = L + L_X + (xa ^ 96);
    lptr xq0 = xp0 + 49152, xq1 = xp1 + 49152, xq2 = xp2 + 49152, xq3 = xp3 + 49152;
    asm volatile("" : "+v"(xq0), "+v"(xq1), "+v"(xq2), "+v"(xq3));
    const int blk = (lane >> 4) & 1, q = (lane & 15) >> 2, pp = lane & 3;
    const unsigned wsw = ((q >> 1) & 1) << 6;
    lptr waX = L + 49152 + (unsigned)((8 * h + q) * 128 + ((56 * hh + 32 * blk + 8 * pp) ^ wsw));
    lptr waR = waX + (L_W - 49152);
    asm volatile("" : "+v"(waX), "+v"(waR));

    bf16x8 xf[28];
    f32x16 accK, accV, accQ;
    bf16x8 q0, q1;
    const unsigned gko = (unsigned)(((b * NH + 2 * g + hh) * NTILE + 4 * r + t) * 2048) + lane * 16;
    const lptr ownK = L + L_K + ((8 + t) * 2 + hh) * 2048 + lane * 16, ownV = L + L_V + ((8 + t) * 2 + hh) * 2048 + lane * 16;
    const int j0 = (2 * w) & 7;
    const bool hv0 = r - 2 + (j0 >> 2) >= 0;
    const unsigned hgo = (unsigned)(((b * NH + 2 * g + hh) * NTILE + (hv0 ? 4 * r - 8 + j0 : 0)) * 2048) + lane * 16;
    const unsigned hfo = (unsigned)((hv0 ? wg - 8 * (2 - (j0 >> 2)) : wg) * 128 + (hh * 4 + ((j0 + (lane & 1)) & 3)) * 8);
    u32x4 hk[4];
    u64 kflag = 0;

    auto ldk = [&](int n, int ks, bf16x8& wf) {
        const int pass = n / 7, tau = n % 7;
        const lptr wbase = (wslot(n) >= L_W ? waR + (wslot(n) - L_W) : waX + (wslot(n) - 49152)) + ks * 2048;
        if (pass == 0) {
            if (n % 6 < 3) xf[4 * tau + ks] = lds_b128((ks == 0 ? xp0 : ks == 1 ? xp1 : ks == 2 ? xp2 : xp3) + (n % 6) * 16384);
            else xf[4 * tau + ks] = lds_b128((ks == 0 ? xq0 : ks == 1 ? xq1 : ks == 2 ? xq2 : xq3) + (n % 6 - 3) * 16384);
        }
        wf = tr_frag(wbase, wbase + 512);
    };
    auto mma = [&](int n, int ks, const bf16x8& wf) {
        const int pass = n / 7, tau = n % 7;
        const bool first = tau == 0 && ks == 0;
        const f32x16 zero = {};
        if (pass == 0) accK = MFMA(wf, xf[4 * tau + ks], first ? zero : accK);
        else if (pass == 1) accV = MFMA(xf[4 * tau + ks], wf, first ? zero : accV);
        else accQ = MFMA(wf, xf[4 * tau + ks], first ? zero : accQ);
    };
    u32x4 e0, e1;
    auto hook = [&](int it, int k) {
        if (it == 3 && k == 5) {
            e0 = pack8u(accK, 0); e1 = pack8u(accK, 1);
            e1[2] = h ? 0u : e1[2]; e1[3] = h ? 0u : e1[3];
        }
        if (it == 3 && k == 6) { lds_w128(ownK, e0); lds_w128(ownK + 1024, e1); }
        if (it == 3 && k == 7) {
            __builtin_amdgcn_raw_buffer_store_b128(e0, rs, WS_K + gko, 0, AUX_SC1);
            __builtin_amdgcn_raw_buffer_store_b128(e1, rs, WS_K + gko + 1024, 0, AUX_SC1);
        }
        if (it == 7 && k == 0)
            flag_store(rs, WS_FK + wg * 128 + w * 8, lane, epoch);
        if (it == 7 && k == 1) {
            e0 = pack8u(accV, 0); e1 = pack8u(accV, 1);
            const unsigned vv = (r31 == DH) ? 0x3F803F80u : 0u;
#pragma unroll
            for (int i = 0; i < 4; ++i) { e0[i] = r31 >= DH ? vv : e0[i]; e1[i] = r31 >= DH ? vv : e1[i]; }
        }
        if (it == 7 && k == 2) { lds_w128(ownV, e0); lds_w128(ownV + 1024, e1); }
        if (it == 7 && k == 3) {
            __builtin_amdgcn_raw_buffer_store_b128(e0, rs, WS_V + gko, 0, AUX_SC1);
            __builtin_amdgcn_raw_buffer_store_b128(e1, rs, WS_V + gko + 1024, 0, AUX_SC1);
        }
        if (it == 9 && k == 0) kflag = __hip_atomic_load((const GAS u64*)(ws + WS_FK + hfo), RLX_AGENT);
    };

#define FS(i)
    bf16x8 wf[2][8];
    waitv_bar<12>();
    asm volatile("" :: "v"(dummy));
#pragma unroll
    for (int k = 0; k < 8; ++k) ldk(k >> 2, k & 3, wf[0][k]);
    auto run_item = [&](auto IT) {
        constexpr int it = decltype(IT)::value;
        constexpr int cnt = it == 0 ? 6 : it == 1 ? 4 : it <= 3 ? 2 : it <= 6 ? 6 : it == 7 ? 3 : it == 8 ? 4 : it == 9 ? 3 : -1;
        waitv_bar<cnt>();
        FS(it);
        if (it == 10) {
            if (hv0) {
                if (!__all(lane >= 2 || kflag == epoch)) wait_flags(ws, WS_FK + hfo, lane < 2, epoch);
                asm volatile("" ::: "memory");
#pragma unroll
                for (int k = 0; k < 4; ++k) hk[k] = __builtin_amdgcn_raw_buffer_load_b128(rs, WS_K + hgo + k * 1024, 0, AUX_SC1);
            } else {
#pragma unroll
                for (int k = 0; k < 4; ++k) hk[k] = (u32x4){0u, 0u, 0u, 0u};
            }
        }
        SBAR();
#pragma unroll
        for (int k = 0; k < 8; ++k) {
            const int n = 2 * it + (k >> 2);
            if (n < 21) mma(n, k & 3, wf[it & 1][k]);
            if (2 * it + 2 + (k >> 2) < 21) ldk(2 * it + 2 + (k >> 2), k & 3, wf[(it + 1) & 1][k]);
            {
                constexpr int np = it == 0 ? 4 : it <= 2 ? 2 : it == 3 ? 4 : it <= 5 ? 2 : it == 6 ? 1 : 0;
                if (np == 4) { if ((k & 1) == 0 && !(it == 3)) issue_nth(it, k >> 1); if (it == 3 && k < 4) issue_nth(it, k); }
                else if (np == 2) { if ((k & 3) == 1) issue_nth(it, k >> 2); }
                else if (np == 1) { if (k == 1) issue_nth(it, 0); }
            }
            hook(it, k);
            SBAR();
        }
    };
    run_item(std::integral_constant<int, 0>{}); run_item(std::integral_constant<int, 1>{}); run_item(std::integral_constant<int, 2>{});
    run_item(std::integral_constant<int, 3>{}); run_item(std::integral_constant<int, 4>{}); run_item(std::integral_constant<int, 5>{});
    run_item(std::integral_constant<int, 6>{}); run_item(std::integral_constant<int, 7>{}); run_item(std::integral_constant<int, 8>{});
    run_item(std::integral_constant<int, 9>{}); run_item(std::integral_constant<int, 10>{});
    q0 = pack8(accQ, 0);
    { u32x4 e = pack8u(accQ, 1); e[2] = h ? 0u : e[2]; e[3] = h ? 0u : e[3]; q1 = __builtin_bit_cast(bf16x8, e); }
    FS(11);
    STAMP(12);

    if (hv0) WAITV(4); else WAITV(0);
    flag_store(rs, WS_FV + wg * 128 + w * 8, lane, epoch);
    LBAR();
    STAMP(13);
    const int ct = g & 3, rb3 = 2 * r + (g >> 2);
    const char* wob = (b ? woB : woA) + ct * 256;
    const unsigned wo3 = (unsigned)((w * 56 + (lane >> 3)) * XROW + (((lane & 7) ^ (((lane >> 4) & 1) << 2)) << 4));
    {
        const lptr lo = L + L_K + (j0 * 2 + hh) * 2048 + lane * 16;
        lds_w128(lo, hk[0]); lds_w128(lo + 1024, hk[1]); lds_w128(lo + 4096, hk[2]); lds_w128(lo + 4096 + 1024, hk[3]);
    }
    LBAR();
    STAMP(14);
#define WO0(i0, i1) do { _Pragma("unroll") for (int i_ = (i0); i_ < (i1); ++i_) glds16(wo3 + i_ * 8 * XROW, wob, lbase + L_WO0 + (w * 7 + i_) * 1024); } while (0)
#define WO1(i0, i1) do { if (ct != 3) { _Pragma("unroll") for (int i_ = (i0); i_ < (i1); ++i_) glds16(wo3 + 128 + i_ * 8 * XROW, wob, lbase + L_WO1 + (w * 7 + i_) * 1024); } } while (0)
    const float NEG = -__builtin_inff();
    const lptr kb = L + L_K + (t * 2 + hh) * 2048 + lane * 16;
    const lptr vb = L + L_V + (t * 2 + hh) * 2048 + lane * 16;
    f32x16 o = {};
    {
        f32x16 sc[9];
        u64 vflag = __hip_atomic_load((const GAS u64*)(ws + WS_FV + hfo), RLX_AGENT);
#pragma unroll
        for (int j = 0; j < 9; ++j) {
            const bf16x8 k0 = lds_b128(kb + j * 4096), k1 = lds_b128(kb + j * 4096 + 1024);
            f32x16 z = {};
            z = MFMA(k0, q0, z);
            sc[j] = MFMA(k1, q1, z);
        }
        WO0(0, 4);
        if (__builtin_expect(r < 2, 0)) {
            asm volatile("" ::: "memory");
#pragma unroll
            for (int j = 0; j < 8; ++j)
                if (4 * r - 8 + t + j < 0) {
                    asm volatile("" ::: "memory");
#pragma unroll
                    for (int i = 0; i < 16; ++i) sc[j][i] = NEG;
                }
        }
#pragma unroll
        for (int i = 0; i < 16; ++i) {
            const int c = (i & 3) + 8 * (i >> 2) + 4 * h;
            sc[8][i] = c > r31 ? sc[0][i] : sc[8][i];
        }
        float m = NEG;
#pragma unroll
        for (int j = 1; j < 9; ++j)
#pragma unroll
            for (int i = 0; i < 16; ++i) m = __builtin_fmaxf(m, sc[j][i]);
        {
            auto rr = __builtin_amdgcn_permlane32_swap(__float_as_uint(m), __float_as_uint(m), false, false);
            m = __builtin_fmaxf(__uint_as_float(rr[0]), __uint_as_float(rr[1]));
        }
        WO0(4, 7);
        u32x4 hv[4];
        if (hv0) {
            if (!__all(lane >= 2 || vflag == epoch)) wait_flags(ws, WS_FV + hfo, lane < 2, epoch);
            asm volatile("" ::: "memory");
#pragma unroll
            for (int k = 0; k < 4; ++k) hv[k] = __builtin_amdgcn_raw_buffer_load_b128(rs, WS_V + hgo + k * 1024, 0, AUX_SC1);
        } else {
#pragma unroll
            for (int k = 0; k < 4; ++k) hv[k] = (u32x4){0u, 0u, 0u, 0u};
        }
        STAMP(15);
        const float nm = -m * SCALE_LOG2E;
        bf16x8 pa[9], pb[9];
#define EXP_TILE(j) do { _Pragma("unroll") for (int i_ = 0; i_ < 16; ++i_) sc[j][i_] = __builtin_amdgcn_exp2f(__builtin_fmaf(sc[j][i_], SCALE_LOG2E, nm)); pa[j] = pack8(sc[j], 0); pb[j] = pack8(sc[j], 1); } while (0)
#define PV_TILE(j) do { const bf16x8 v0_ = lds_b128(vb + (j) * 4096), v1_ = lds_b128(vb + (j) * 4096 + 1024); o = MFMA(v0_, pa[j], o); o = MFMA(v1_, pb[j], o); } while (0)
        {
#pragma unroll
            for (int i = 0; i < 16; ++i) sc[8][i] = __builtin_amdgcn_exp2f(__builtin_fmaf(sc[8][i], SCALE_LOG2E, nm));
#pragma unroll
            for (int i = 0; i < 16; ++i) {
                const int c = (i & 3) + 8 * (i >> 2) + 4 * h;
                sc[0][i] = c > r31 ? sc[8][i] : 0.f;
                sc[8][i] = c > r31 ? 0.f : sc[8][i];
            }
            pa[8] = pack8(sc[8], 0); pb[8] = pack8(sc[8], 1); pa[0] = pack8(sc[0], 0); pb[0] = pack8(sc[0], 1);
        }
        PV_TILE(8);
        EXP_TILE(7); EXP_TILE(6); EXP_TILE(5); EXP_TILE(4);
        {
            const lptr lo = L + L_V + (j0 * 2 + hh) * 2048 + lane * 16;
            lds_w128(lo, hv[0]); lds_w128(lo + 1024, hv[1]); lds_w128(lo + 4096, hv[2]); lds_w128(lo + 4096 + 1024, hv[3]);
        }
        LBAR();
        STAMP(16);
        WO1(0, 2); PV_TILE(7); EXP_TILE(3); WO1(2, 4); PV_TILE(6); EXP_TILE(2); WO1(4, 6); PV_TILE(5); EXP_TILE(1); WO1(6, 7); PV_TILE(4); PV_TILE(0);
        PV_TILE(3); PV_TILE(2); PV_TILE(1);
    }
    STAMP(17);
    {
        float l = o[12];
        auto rr = __builtin_amdgcn_permlane32_swap(__float_as_uint(l), __float_as_uint(l), false, false);
        l = __uint_as_float(rr[1]);
        const float inv = 1.0f / l;
#pragma unroll
        for (int i = 0; i < 16; ++i) o[i] *= inv;
        if (h == 1) {
#pragma unroll
            for (int i = 12; i < 16; ++i) o[i] = 0.f;
        }
        const unsigned go = WS_O + (unsigned)(((b * NTILE + 4 * r + t) * NH + 2 * g + hh) * 2048) + lane * 16;
        __builtin_amdgcn_raw_buffer_store_b128(pack8u(o, 0), rs, go, 0, AUX_SC1);
        __builtin_amdgcn_raw_buffer_store_b128(pack8u(o, 1), rs, go + 1024, 0, AUX_SC1);
        WAITV(0);
        flag_store(rs, WS_FO + wg * 128 + w * 8, lane, epoch);
    }
    STAMP(18);

    const int tt = w & 1, kq = w >> 1;
    const int nimg = ct == 3 ? 1 : 2;
    {
        const int ts = 2 * (g >> 2) + tt;
        wait_flags(ws, WS_FO + (unsigned)(((b * 16 + r) * 8 + 2 * kq + ((lane >> 1) & 1)) * 128 + ((lane & 1) * 4 + ts) * 8), lane < 4, epoch);
    }
    STAMP(19);
    bf16x8 of[8];
    {
        const unsigned go = WS_O + (unsigned)(((b * NTILE + 2 * rb3 + tt) * NH + 4 * kq) * 2048) + lane * 16;
#pragma unroll
        for (int i = 0; i < 8; ++i) of[i] = __builtin_bit_cast(bf16x8, __builtin_amdgcn_raw_buffer_load_b128(rs, go + i * 1024, 0, AUX_SC1));
    }
    WAITV_BAR(8);
    STAMP(20);
    f32x16 acc3[4] = {};
    {
        const int row0 = 4 * kq * DH + 4 * h + q;
        int rl = row0 + 3 * DH + 16 + 8; rl = rl > 447 ? (rl & 3) + 444 : rl;
        lptr pn[4], pl[4];
#pragma unroll
        for (int i = 0; i < 4; ++i) {
            const int n = (kq + i) & 3;
            const unsigned co = (n >> 1) * (L_WO1 - L_WO0) + ((((n & 1) << 6) + 32 * blk + 8 * pp) ^ wsw);
            pn[i] = L + L_WO0 + row0 * 128 + co; pl[i] = L + L_WO0 + rl * 128 + co;
        }
#pragma unroll
        for (int i = 0; i < 4; ++i)
#pragma unroll
            for (int s = 0; s < 2; ++s) {
                const int ro = (i * DH + 16 * s) * 128;
                const bool last = i == 3 && s == 1;
#pragma unroll
                for (int n = 0; n < 4; ++n) acc3[n] = MFMA(tr_frag(pn[n] + ro, last ? pl[n] : pn[n] + ro + 1024), of[2 * i + s], acc3[n]);
            }
    }
    STAMP(21);
    LBAR();
    {
#pragma unroll
        for (int i = 1; i < 4; ++i) {
            const lptr dst = L + tt * 49152 + ((kq + i) & 3) * 12288 + (i - 1) * 4096 + lane * 16;
#pragma unroll
            for (int e = 0; e < 4; ++e) {
                const f32x4 v = {acc3[i][4 * e], acc3[i][4 * e + 1], acc3[i][4 * e + 2], acc3[i][4 * e + 3]};
                *(LAS f32x4*)(dst + e * 1024) = v;
            }
        }
    }
    LBAR();
    f32x16 fin = acc3[0];
    {
        const lptr xr = L + tt * 49152 + kq * 12288 + lane * 16;
#pragma unroll
        for (int k = 0; k < 3; ++k)
#pragma unroll
            for (int e = 0; e < 4; ++e) {
                const f32x4 v = *(const LAS f32x4*)(xr + k * 4096 + e * 1024);
                fin[4 * e] += v[0]; fin[4 * e + 1] += v[1]; fin[4 * e + 2] += v[2]; fin[4 * e + 3] += v[3];
            }
    }
    if (kq < 2 * nimg) {
        char* orow = out + (size_t)(64 * rb3 + 32 * tt + r31) * OROW + (b * D_ + 128 * ct + 32 * kq) * 2;
#pragma unroll
        for (int qq = 0; qq < 4; qq += 2) {
            unsigned ax = pk2(fin[4 * qq], fin[4 * qq + 1]), ay = pk2(fin[4 * qq + 2], fin[4 * qq + 3]);
            unsigned bx = pk2(fin[4 * qq + 4], fin[4 * qq + 5]), by = pk2(fin[4 * qq + 6], fin[4 * qq + 7]);
            auto r0 = __builtin_amdgcn_permlane32_swap(ax, bx, false, false);
            auto r1 = __builtin_amdgcn_permlane32_swap(ay, by, false, false);
            u32x4 v = {r0[0], r1[0], r0[1], r1[1]};
            *(u32x4*)(orow + (8 * qq + 8 * h) * 2) = v;
        }
    }
    STAMP(22);
    if (wg == 0 && w == 0) {
        const u64 t0 = __builtin_amdgcn_s_memrealtime();
        for (;;) {
            bool ok = true;
#pragma unroll
            for (int k = 0; k < 4; ++k) ok &= __hip_atomic_load((const GAS u64*)(ws + WS_FO + (k * 64 + lane) * 128), RLX_AGENT) == epoch;
            if (__all(ok)) break;
            __builtin_amdgcn_s_sleep(1);
            if (__builtin_amdgcn_s_memrealtime() - t0 > 2000000ull) break;
        }
        if (lane == 0) __hip_atomic_store((GAS u64*)(ws + WS_G), epoch, RLX_AGENT);
    }
    STAMP(23);
}

extern "C" void kernel_launch(void* const* d_in, const int* in_sizes, int n_in, void* d_out, int out_size, void* d_ws, size_t ws_size,
                              hipStream_t stream) {
    (void)hipFuncSetAttribute((const void*)k_fused, hipFuncAttributeMaxDynamicSharedMemorySize, LDS_BYTES);
    k_fused<<<256, 512, LDS_BYTES, stream>>>((const char*)d_in[0], (const char*)d_in[1], (const char*)d_in[2], (const char*)d_in[3],
                                             (const char*)d_in[4], (const char*)d_in[5], (char*)d_out, (char*)d_ws);
}
```

```cpp
#include <hip/hip_runtime.h>
#include <hip/hip_bf16.h>
#include <stdint.h>
#include <type_traits>

typedef __bf16 bf16x8 __attribute__((ext_vector_type(8)));
typedef __bf16 bf16x2 __attribute__((ext_vector_type(2)));
typedef float f32x16 __attribute__((ext_vector_type(16)));
typedef float f32x2 __attribute__((ext_vector_type(2)));
typedef float f32x4 __attribute__((ext_vector_type(4)));
typedef short s16x4 __attribute__((ext_vector_type(4)));
typedef short s16x8 __attribute__((ext_vector_type(8)));
typedef unsigned u32x4 __attribute__((ext_vector_type(4)));
typedef unsigned long long u64;
#define LAS __attribute__((address_space(3)))
#define GAS __attribute__((address_space(1)))
#define DI __device__ __forceinline__
#define MFMA(a, b, c) __builtin_amdgcn_mfma_f32_32x32x16_bf16((a), (b), (c), 0, 0, 0)
#define RLX_AGENT __ATOMIC_RELAXED, __HIP_MEMORY_SCOPE_AGENT

constexpr int S_ = 2048, D_ = 448, NH = 16, DH = 28;
constexpr int XROW = D_ * 2;
constexpr int WROW = 3 * D_ * 2;
constexpr int OROW = 2 * D_ * 2;
constexpr int NTILE = S_ / 32;
constexpr unsigned WS_G = 0;
constexpr unsigned WS_FK = 4096;
constexpr unsigned WS_FV = WS_FK + 256 * 128;
constexpr unsigned WS_FO = WS_FV + 256 * 128;
constexpr unsigned WS_K = 1u << 20;
constexpr unsigned KARR_BYTES = 2u * NH * NTILE * 2048;
constexpr unsigned WS_V = WS_K + KARR_BYTES;
constexpr unsigned WS_O = WS_V + KARR_BYTES;
constexpr unsigned WS_END = WS_O + KARR_BYTES;
constexpr float SCALE_LOG2E = 0.18898223650461362f * 1.4426950408889634f;
constexpr int AUX_SC1 = 16;

DI unsigned pk2(float a, float b) {
    f32x2 p = {a, b};
    return __builtin_bit_cast(unsigned, __builtin_convertvector(p, bf16x2));
}
DI u32x4 pack8u(const f32x16& x, int s) {
    u32x4 w = {pk2(x[8 * s + 0], x[8 * s + 1]), pk2(x[8 * s + 2], x[8 * s + 3]), pk2(x[8 * s + 4], x[8 * s + 5]), pk2(x[8 * s + 6], x[8 * s + 7])};
    return w;
}
DI bf16x8 pack8(const f32x16& x, int s) { return __builtin_bit_cast(bf16x8, pack8u(x, s)); }
DI void glds16(unsigned voff, const void* sbase, unsigned lds_base) {
    unsigned sv;
    asm volatile("s_mov_b32 %0, m0\n\ts_mov_b32 m0, %3\n\ts_nop 0\n\tglobal_load_lds_dwordx4 %1, %2\n\ts_mov_b32 m0, %0"
                 : "=&s"(sv) : "v"(voff), "s"(sbase), "s"(lds_base) : "memory");
}
DI void glds16_nc(unsigned voff, const void* sbase, unsigned lds_base) {
    unsigned sv;
    asm volatile("s_mov_b32 %0, m0\n\ts_mov_b32 m0, %3\n\ts_nop 0\n\tglobal_load_lds_dwordx4 %1, %2\n\ts_mov_b32 m0, %0"
                 : "=&s"(sv) : "v"(voff), "s"(sbase), "s"(lds_base));
}
DI void glds16_sc1(unsigned voff, const void* sbase, unsigned lds_base) {
    unsigned sv;
    asm volatile("s_mov_b32 %0, m0\n\ts_mov_b32 m0, %3\n\ts_nop 0\n\tglobal_load_lds_dwordx4 %1, %2 sc1\n\ts_mov_b32 m0, %0"
                 : "=&s"(sv) : "v"(voff), "s"(sbase), "s"(lds_base) : "memory");
}
typedef LAS char* lptr;
DI bf16x8 lds_b128(lptr p) { return *(const LAS bf16x8*)p; }
DI void lds_w128(lptr p, u32x4 v) { *(LAS u32x4*)p = v; }
DI s16x4 lds_tr(lptr p) { return __builtin_amdgcn_ds_read_tr16_b64_v4i16((LAS s16x4*)p); }
DI bf16x8 cat8(s16x4 lo, s16x4 hi) { s16x8 v = __builtin_shufflevector(lo, hi, 0, 1, 2, 3, 4, 5, 6, 7); return __builtin_bit_cast(bf16x8, v); }
DI bf16x8 tr_frag(lptr a0, lptr a1) { return cat8(lds_tr(a0), lds_tr(a1)); }
#define SBAR() __builtin_amdgcn_sched_barrier(0)

#define STAMP(i)
#define WAITV(N) asm volatile("s_waitcnt vmcnt(" #N ")" ::: "memory")
#define WAITV_BAR(N) asm volatile("s_waitcnt vmcnt(" #N ") lgkmcnt(0)\n\ts_barrier" ::: "memory")
#define LBAR() asm volatile("s_waitcnt lgkmcnt(0)\n\ts_barrier" ::: "memory")
template <int N> DI void waitv_bar() {
    if constexpr (N == 2) WAITV_BAR(2); else if constexpr (N == 3) WAITV_BAR(3); else if constexpr (N == 4) WAITV_BAR(4);
    else if constexpr (N == 6) WAITV_BAR(6); else if constexpr (N == 12) WAITV_BAR(12); else LBAR();
}

DI void flag_store(__amdgpu_buffer_rsrc_t rs, unsigned off, int lane, u64 e) {
    typedef unsigned u32x2 __attribute__((ext_vector_type(2)));
    const u32x2 v = {(unsigned)e, (unsigned)(e >> 32)};
    __builtin_amdgcn_raw_buffer_store_b64(v, rs, lane == 0 ? off : 0xF0000000u, 0, AUX_SC1);
}
DI void wait_flags(const char* ws, unsigned off, bool active, u64 e) {
    const GAS u64* p = (const GAS u64*)(ws + off);
    const u64 t0 = __builtin_amdgcn_s_memrealtime();
    for (;;) {
        u64 v = e;
        if (active) v = __hip_atomic_load(p, RLX_AGENT);
        if (__all(v == e)) break;
        __builtin_amdgcn_s_sleep(1);
        if (__builtin_amdgcn_s_memrealtime() - t0 > 2000000ull) break;
    }
    asm volatile("" ::: "memory");
}

constexpr int L_X = 0;
constexpr int L_W = 98304;
constexpr int L_V = 0;
constexpr int L_WO0 = 49152;
constexpr int L_WO1 = 106496;
constexpr int L_K = 114688;
constexpr int LDS_BYTES = 163840;
constexpr int wslot(int n) { return (n >= 12 && n < 18) ? 49152 + (n - 12) * 8192 : L_W + (n % 6) * 8192; }
constexpr int npieces(int n) { return n >= 21 ? 0 : n < 7 ? 3 : 1; }

#define ROT 1

__global__ __launch_bounds__(512, 2) void k_fused(const char* __restrict__ xA, const char* __restrict__ wA, const char* __restrict__ woA,
                                                  const char* __restrict__ xB, const char* __restrict__ wB, const char* __restrict__ woB,
                                                  char* __restrict__ out, char* __restrict__ ws) {
    extern __shared__ __attribute__((aligned(16))) char lds[];
    const u64 epoch = *(const u64*)(ws + WS_G) + 1ull;
    const int xcd = blockIdx.x & 7, idx = blockIdx.x >> 3;
    const int b = xcd >> 2;
    const int r = ((xcd >> 1) & 1) * 8 + (idx >> 2);
    const int g = (xcd & 1) * 4 + (idx & 3);
    const int wg = (b * 16 + r) * 8 + g;
    const int tid = threadIdx.x, lane = tid & 63;
    const int w = __builtin_amdgcn_readfirstlane(tid >> 6);
    const int hh = w >> 2, t = w & 3;
    const char* xb = (b ? xB : xA) + (size_t)r * 128 * XROW;
    const char* wb = (b ? wB : wA) + g * 112;
    const unsigned lbase = (unsigned)(size_t)lds;
    const lptr L = (lptr)lds;
    const __amdgpu_buffer_rsrc_t rs = __builtin_amdgcn_make_buffer_rsrc(ws, 0, WS_END, 0x00020000);

    const unsigned xo0 = (unsigned)((16 * w + (lane >> 3)) * XROW + (((lane & 7) ^ (lane >> 4)) << 4));
    const unsigned xo1 = (unsigned)((16 * w + 8 + (lane >> 3)) * XROW + (((lane & 7) ^ (4 + (lane >> 4))) << 4));
    int wc = (lane & 7) ^ (((lane >> 4) & 1) << 2); wc = wc > 6 ? 6 : wc;
    const unsigned wo0 = (unsigned)((8 * w + (lane >> 3)) * WROW + wc * 16);
    const int rot = ROT ? (2 * (r & 7) + 3 * (g & 3)) % 7 : 0;

    auto issue_piece = [&](int n, int k) {
        const int pass = n / 7, tau = n % 7;
        int s = tau + rot; s = s >= 7 ? s - 7 : s;
        const unsigned poff = pass == 0 ? 896u : pass == 1 ? 1792u : 0u;
        if (k == 0) glds16(wo0 + s * 64 * WROW + poff, wb, lbase + wslot(n) + w * 1024);
        else glds16((k == 1 ? xo0 : xo1) + s * 128, xb, lbase + L_X + (n % 6) * 16384 + w * 2048 + (k - 1) * 1024);
    };
    auto issue_item = [&](int it) {
#pragma unroll
        for (int k = 0; k < npieces(2 * it); ++k) issue_piece(2 * it, k);
#pragma unroll
        for (int k = 0; k < npieces(2 * it + 1); ++k) issue_piece(2 * it + 1, k);
    };
    auto issue_nth = [&](int it, int p) {
        const int n0 = it <= 2 ? 2 * (it + 3) : it == 3 ? 12 : it <= 6 ? 2 * (it + 4) : 21;
        const int n1 = it <= 2 ? n0 + 2 : it == 3 ? 16 : it <= 6 ? n0 + 2 : 21;
        int c = 0;
#pragma unroll
        for (int n = n0; n < n1; ++n)
#pragma unroll
            for (int k = 0; k < npieces(n); ++k) { if (c == p) issue_piece(n, k); ++c; }
    };
    issue_item(0); issue_item(1); issue_item(2);

    const int r31 = lane & 31, h = lane >> 5;
    const int xrow = 32 * t + r31;
    const unsigned xa = (unsigned)(xrow * 128 + ((h ^ ((xrow >> 1) & 7)) << 4));
    lptr xp0 = L + L_X + xa, xp1 = L + L_X + (xa ^ 32), xp2 = L + L_X + (xa ^ 64), xp3 = L + L_X + (xa ^ 96);
    lptr xq0 = xp0 + 49152, xq1 = xp1 + 49152, xq2 = xp2 + 49152, xq3 = xp3 + 49152;
    asm volatile("" : "+v"(xq0), "+v"(xq1), "+v"(xq2), "+v"(xq3));
    const int blk = (lane >> 4) & 1, q = (lane & 15) >> 2, pp = lane & 3;
    const unsigned wsw = ((q >> 1) & 1) << 6;
    lptr waX = L + 49152 + (unsigned)((8 * h + q) * 128 + ((56 * hh + 32 * blk + 8 * pp) ^ wsw));
    lptr waR = waX + (L_W - 49152);
    asm volatile("" : "+v"(waX), "+v"(waR));

    bf16x8 xf[28];
    f32x16 accK, accV, accQ;
    bf16x8 q0, q1;
    const unsigned gko = (unsigned)(((b * NH + 2 * g + hh) * NTILE + 4 * r + t) * 2048) + lane * 16;
    const lptr ownK = L + L_K + ((8 + t) * 2 + hh) * 2048 + lane * 16, ownV = L + L_V + ((8 + t) * 2 + hh) * 2048 + lane * 16;
    const int j0 = (2 * w) & 7;
    const bool hv0 = r - 2 + (j0 >> 2) >= 0;
    const unsigned hgo = (unsigned)(((b * NH + 2 * g + hh) * NTILE + (hv0 ? 4 * r - 8 + j0 : 0)) * 2048) + lane * 16;
    const unsigned hfo = (unsigned)((hv0 ? wg - 8 * (2 - (j0 >> 2)) : wg) * 128 + (hh * 4 + ((j0 + (lane & 1)) & 3)) * 8);
    u32x4 hk[4];
    u64 kflag = 0;

    auto ldk = [&](int n, int ks, bf16x8& wf) {
        const int pass = n / 7, tau = n % 7;
        const lptr wbase = (wslot(n) >= L_W ? waR + (wslot(n) - L_W) : waX + (wslot(n) - 49152)) + ks * 2048;
        if (pass == 0) {
            if (n % 6 < 3) xf[4 * tau + ks] = lds_b128((ks == 0 ? xp0 : ks == 1 ? xp1 : ks == 2 ? xp2 : xp3) + (n % 6) * 16384);
            else xf[4 * tau + ks] = lds_b128((ks == 0 ? xq0 : ks == 1 ? xq1 : ks == 2 ? xq2 : xq3) + (n % 6 - 3) * 16384);
        }
        wf = tr_frag(wbase, wbase + 512);
    };
    auto mma = [&](int n, int ks, const bf16x8& wf) {
        const int pass = n / 7, tau = n % 7;
        const bool first = tau == 0 && ks == 0;
        const f32x16 zero = {};
        if (pass == 0) accK = MFMA(wf, xf[4 * tau + ks], first ? zero : accK);
        else if (pass == 1) accV = MFMA(xf[4 * tau + ks], wf, first ? zero : accV);
        else accQ = MFMA(wf, xf[4 * tau + ks], first ? zero : accQ);
    };
    u32x4 e0, e1;
    const int ct = g & 3, rb3 = 2 * r + (g >> 2);
    const int nimg = ct == 3 ? 1 : 2;
    const char* wob = (b ? woB : woA) + ct * 256;
    const unsigned wo3 = (unsigned)((lane >> 3) * XROW + (((lane & 7) ^ (((lane >> 4) & 1) << 2)) << 4));
#define WOP(i) glds16_nc(wo3 + (6 * w + (i)) * 8 * XROW, wob, lbase + L_WO0 + (6 * w + (i)) * 1024)
    auto hook = [&](int it, int k) {
        if (it == 3 && k == 5) {
            e0 = pack8u(accK, 0); e1 = pack8u(accK, 1);
            e1[2] = h ? 0u : e1[2]; e1[3] = h ? 0u : e1[3];
        }
        if (it == 3 && k == 6) { lds_w128(ownK, e0); lds_w128(ownK + 1024, e1); }
        if (it == 3 && k == 7) {
            __builtin_amdgcn_raw_buffer_store_b128(e0, rs, WS_K + gko, 0, AUX_SC1);
            __builtin_amdgcn_raw_buffer_store_b128(e1, rs, WS_K + gko + 1024, 0, AUX_SC1);
        }
        if (it == 7 && k == 0)
            flag_store(rs, WS_FK + wg * 128 + w * 8, lane, epoch);
        if (it == 7 && k == 1) {
            e0 = pack8u(accV, 0); e1 = pack8u(accV, 1);
            unsigned vv = (r31 == DH) ? 0x3F803F80u : 0u, keep = r31 < DH ? ~0u : 0u;
            asm volatile("" : "+v"(vv), "+v"(keep));
#pragma unroll
            for (int i = 0; i < 4; ++i) { e0[i] = (e0[i] & keep) | vv; e1[i] = (e1[i] & keep) | vv; }
        }
        if (it == 7 && k == 2) { lds_w128(ownV, e0); lds_w128(ownV + 1024, e1); }
        if (it == 7 && k == 3) {
            __builtin_amdgcn_raw_buffer_store_b128(e0, rs, WS_V + gko, 0, AUX_SC1);
            __builtin_amdgcn_raw_buffer_store_b128(e1, rs, WS_V + gko + 1024, 0, AUX_SC1);
        }
        if (it == 8 && (k & 1) == 1 && k >= 3) WOP((k - 3) >> 1);
        if (it == 9 && (k & 1) == 1 && k >= 3) WOP(3 + ((k - 3) >> 1));
        if (it == 9 && k == 0) kflag = __hip_atomic_load((const GAS u64*)(ws + WS_FK + hfo), RLX_AGENT);
    };

#define PS(i)
#define FS(i)
    bf16x8 wf[2][8];
    waitv_bar<12>();
#pragma unroll
    for (int k = 0; k < 8; ++k) ldk(k >> 2, k & 3, wf[0][k]);
    auto run_item = [&](auto IT) {
        constexpr int it = decltype(IT)::value;
        constexpr int cnt = it == 0 ? 6 : it == 1 ? 4 : it <= 3 ? 2 : it <= 6 ? 6 : it == 7 ? 3 : it == 8 ? 4 : it == 9 ? 6 : -1;
        waitv_bar<cnt>();
        FS(it);
        if (it == 10) {
            if (hv0) {
                if (!__all(lane >= 2 || kflag == epoch)) wait_flags(ws, WS_FK + hfo, lane < 2, epoch);
                asm volatile("" ::: "memory");
#pragma unroll
                for (int k = 0; k < 4; ++k) hk[k] = __builtin_amdgcn_raw_buffer_load_b128(rs, WS_K + hgo + k * 1024, 0, AUX_SC1);
            } else {
#pragma unroll
                for (int k = 0; k < 4; ++k) hk[k] = (u32x4){0u, 0u, 0u, 0u};
            }
        }
        SBAR();
#pragma unroll
        for (int k = 0; k < 8; ++k) {
            const int n = 2 * it + (k >> 2);
            if (n < 21) mma(n, k & 3, wf[it & 1][k]);
            if (2 * it + 2 + (k >> 2) < 21) ldk(2 * it + 2 + (k >> 2), k & 3, wf[(it + 1) & 1][k]);
            {
                constexpr int np = it == 0 ? 4 : it <= 2 ? 2 : it == 3 ? 4 : it <= 5 ? 2 : it == 6 ? 1 : 0;
                if (np == 4) { if ((k & 1) == 0 && !(it == 3)) issue_nth(it, k >> 1); if (it == 3 && k < 4) issue_nth(it, k); }
                else if (np == 2) { if ((k & 3) == 1) issue_nth(it, k >> 2); }
                else if (np == 1) { if (k == 1) issue_nth(it, 0); }
            }
            hook(it, k);
            SBAR();
        }
    };
    run_item(std::integral_constant<int, 0>{}); run_item(std::integral_constant<int, 1>{}); run_item(std::integral_constant<int, 2>{});
    run_item(std::integral_constant<int, 3>{}); run_item(std::integral_constant<int, 4>{}); run_item(std::integral_constant<int, 5>{});
    run_item(std::integral_constant<int, 6>{}); run_item(std::integral_constant<int, 7>{}); run_item(std::integral_constant<int, 8>{});
    run_item(std::integral_constant<int, 9>{}); run_item(std::integral_constant<int, 10>{});
#pragma unroll
    for (int i = 0; i < 16; ++i) accQ[i] *= SCALE_LOG2E;
    q0 = pack8(accQ, 0);
    { u32x4 e = pack8u(accQ, 1); e[2] = h ? 0u : e[2]; e[3] = h ? 0u : e[3]; q1 = __builtin_bit_cast(bf16x8, e); }
    FS(11);
    PS(0);

    if (hv0) WAITV(11); else WAITV(7);
    flag_store(rs, WS_FV + wg * 128 + w * 8, lane, epoch);
    LBAR();
    PS(1);
    {
        const lptr lo = L + L_K + (j0 * 2 + hh) * 2048 + lane * 16;
        lds_w128(lo, hk[0]); lds_w128(lo + 1024, hk[1]); lds_w128(lo + 4096, hk[2]); lds_w128(lo + 4096 + 1024, hk[3]);
    }
    LBAR();
    PS(2);
    glds16_nc(wo3 + (48 + w) * 8 * XROW, wob, lbase + L_WO0 + (48 + w) * 1024);
    const float NEG = -__builtin_inff();
    const lptr kb = L + L_K + (t * 2 + hh) * 2048 + lane * 16;
    const lptr vb = L + L_V + (t * 2 + hh) * 2048 + lane * 16;
    f32x16 o = {};
    auto attn = [&](auto EDGE_) {
        constexpr bool EDGE = decltype(EDGE_)::value;
        bf16x8 kf0[9], kf1[9];
#pragma unroll
        for (int j = 0; j < 9; ++j) { kf0[j] = lds_b128(kb + j * 4096); kf1[j] = lds_b128(kb + j * 4096 + 1024); }
        const f32x16 zero = {};
#define S_TILE(dst, j) do { dst = MFMA(kf0[j], q0, zero); dst = MFMA(kf1[j], q1, dst); } while (0)
#define MX_TILE(src, j) do { if (!(EDGE && 4 * r - 8 + t + (j) < 0)) { _Pragma("unroll") for (int i_ = 0; i_ < 16; i_ += 4) { m0 = __builtin_fmaxf(__builtin_fmaxf(m0, src[i_]), src[i_ + 1]); m1 = __builtin_fmaxf(__builtin_fmaxf(m1, src[i_ + 2]), src[i_ + 3]); } } } while (0)
        f32x16 s0, mg, y0, y1, y2;
        float m0 = NEG, m1 = NEG;
        S_TILE(s0, 0); S_TILE(mg, 8); S_TILE(y0, 1); S_TILE(y1, 2);
        SBAR();
        S_TILE(y2, 3);
        if (EDGE && 4 * r - 8 + t < 0) {
#pragma unroll
            for (int i = 0; i < 16; ++i) s0[i] = NEG;
        }
#pragma unroll
        for (int i = 0; i < 16; ++i) { const int c = (i & 3) + 8 * (i >> 2) + 4 * h; mg[i] = c > r31 ? s0[i] : mg[i]; }
#pragma unroll
        for (int i = 0; i < 16; i += 4) { m0 = __builtin_fmaxf(__builtin_fmaxf(m0, mg[i]), mg[i + 1]); m1 = __builtin_fmaxf(__builtin_fmaxf(m1, mg[i + 2]), mg[i + 3]); }
        SBAR();
        MX_TILE(y0, 1); SBAR();
        S_TILE(y0, 4); MX_TILE(y1, 2); SBAR();
        S_TILE(y1, 5); MX_TILE(y2, 3); SBAR();
        S_TILE(y2, 6); MX_TILE(y0, 4); SBAR();
        S_TILE(y0, 7); MX_TILE(y1, 5); SBAR();
        MX_TILE(y2, 6); SBAR();
        MX_TILE(y0, 7);
        float m = __builtin_fmaxf(m0, m1);
        {
            auto rr = __builtin_amdgcn_permlane32_swap(__float_as_uint(m), __float_as_uint(m), false, false);
            m = __builtin_fmaxf(__uint_as_float(rr[0]), __uint_as_float(rr[1]));
        }
        f32x16 cm;
#pragma unroll
        for (int i = 0; i < 16; ++i) cm[i] = -m;
#define SM_TILE(dst, j) do { dst = MFMA(kf0[j], q0, cm); dst = MFMA(kf1[j], q1, dst); } while (0)
        SM_TILE(y0, 7); SM_TILE(y1, 6);
        const u64 vflag = __hip_atomic_load((const GAS u64*)(ws + WS_FV + hfo), RLX_AGENT);
        PS(3);
        bf16x8 pa[9], pb[9];
#define EX_TILE(src, j) do { if (EDGE && 4 * r - 8 + t + (j) < 0) { _Pragma("unroll") for (int i_ = 0; i_ < 16; ++i_) src[i_] = 0.f; } else { _Pragma("unroll") for (int i_ = 0; i_ < 16; ++i_) src[i_] = __builtin_amdgcn_exp2f(src[i_]); } \
        pa[j] = pack8(src, 0); pb[j] = pack8(src, 1); } while (0)
#define PV_TILE(j) do { const bf16x8 v0_ = lds_b128(vb + (j) * 4096), v1_ = lds_b128(vb + (j) * 4096 + 1024); o = MFMA(v0_, pa[j], o); o = MFMA(v1_, pb[j], o); } while (0)
        {
#pragma unroll
            for (int i = 0; i < 16; ++i) mg[i] = __builtin_amdgcn_exp2f(mg[i] - m);
#pragma unroll
            for (int i = 0; i < 16; ++i) {
                const int c = (i & 3) + 8 * (i >> 2) + 4 * h;
                s0[i] = c > r31 ? mg[i] : 0.f;
                mg[i] = c > r31 ? 0.f : mg[i];
            }
            pa[8] = pack8(mg, 0); pb[8] = pack8(mg, 1); pa[0] = pack8(s0, 0); pb[0] = pack8(s0, 1);
        }
        SBAR();
        SM_TILE(y2, 5); PV_TILE(8);
        EX_TILE(y0, 7); SBAR();
        SM_TILE(y0, 4);
        EX_TILE(y1, 6); SBAR();
        u32x4 hv[4];
        if (hv0) {
            if (!__all(lane >= 2 || vflag == epoch)) wait_flags(ws, WS_FV + hfo, lane < 2, epoch);
            asm volatile("" ::: "memory");
#pragma unroll
            for (int k = 0; k < 4; ++k) hv[k] = __builtin_amdgcn_raw_buffer_load_b128(rs, WS_V + hgo + k * 1024, 0, AUX_SC1);
        } else {
#pragma unroll
            for (int k = 0; k < 4; ++k) hv[k] = (u32x4){0u, 0u, 0u, 0u};
        }
        SM_TILE(y1, 3);
        EX_TILE(y2, 5); SBAR();
        SM_TILE(y2, 2);
        EX_TILE(y0, 4); SBAR();
        SM_TILE(y0, 1);
        EX_TILE(y1, 3);
        {
            const lptr lo = L + L_V + (j0 * 2 + hh) * 2048 + lane * 16;
            lds_w128(lo, hv[0]); lds_w128(lo + 1024, hv[1]); lds_w128(lo + 4096, hv[2]); lds_w128(lo + 4096 + 1024, hv[3]);
        }
        LBAR();
        PS(4);
        PV_TILE(7); PV_TILE(6); PV_TILE(5);
        EX_TILE(y2, 2); SBAR();
        PV_TILE(4); PV_TILE(0); PV_TILE(3);
        EX_TILE(y0, 1); SBAR();
        PV_TILE(2); PV_TILE(1);
    };
    if (__builtin_expect(r >= 2, 1)) attn(std::false_type{}); else attn(std::true_type{});
    PS(5);
    {
        float l = o[12];
        auto rr = __builtin_amdgcn_permlane32_swap(__float_as_uint(l), __float_as_uint(l), false, false);
        l = __uint_as_float(rr[1]);
        const float inv = 1.0f / l;
#pragma unroll
        for (int i = 0; i < 16; ++i) o[i] *= inv;
        if (h == 1) {
#pragma unroll
            for (int i = 12; i < 16; ++i) o[i] = 0.f;
        }
        const unsigned go = WS_O + (unsigned)(((b * NTILE + 4 * r + t) * NH + 2 * g + hh) * 2048) + lane * 16;
        __builtin_amdgcn_raw_buffer_store_b128(pack8u(o, 0), rs, go, 0, AUX_SC1);
        __builtin_amdgcn_raw_buffer_store_b128(pack8u(o, 1), rs, go + 1024, 0, AUX_SC1);
        if (nimg == 2) {
#pragma unroll
            for (int i = 0; i < 7; ++i) glds16_nc(wo3 + 128 + (7 * w + i) * 8 * XROW, wob, lbase + L_WO1 + (7 * w + i) * 1024);
            WAITV(7);
        } else WAITV(0);
        flag_store(rs, WS_FO + wg * 128 + w * 8, lane, epoch);
    }
    PS(6);

    const int tt = w & 1, nt = w >> 1;
    bf16x8 wfr[32];
    const int row0 = 4 * h + q;
    const lptr pw = L + L_WO0 + (nt >> 1) * (L_WO1 - L_WO0) + row0 * 128 + ((((nt & 1) << 6) + 32 * blk + 8 * pp) ^ wsw);
    const lptr pwl = pw + (h ? (444 - 15 * DH - 24 - 4) * 128 : 0);
    auto ld_wfr = [&]() {
#pragma unroll
        for (int hd = 0; hd < 16; ++hd)
#pragma unroll
            for (int s = 0; s < 2; ++s) {
                const int ro = (hd * DH + 16 * s) * 128;
                if (hd == 15 && s == 1) wfr[2 * hd + s] = tr_frag(pw + ro, pwl + ro + 1024);
                else wfr[2 * hd + s] = tr_frag(pw + ro, pw + ro + 1024);
            }
    };
    LBAR();
    if (nt < 2) ld_wfr();
    LBAR();
    PS(7);
    wait_flags(ws, WS_FO + (unsigned)(((b * 16 + r) * 8 + w) * 128 + ((lane & 1) * 4 + 2 * (g >> 2) + ((lane >> 1) & 1)) * 8), lane < 4, epoch);
    {
        const char* ob = ws + WS_O;
        const unsigned go = (unsigned)(((b * NTILE + 2 * rb3) * NH + 2 * w) * 2048) + lane * 16;
#pragma unroll
        for (int i = 0; i < 4; ++i) {
            glds16_sc1(go + i * 1024, ob, lbase + w * 4096 + i * 1024);
            glds16_sc1(go + NH * 2048 + i * 1024, ob, lbase + 32768 + w * 4096 + i * 1024);
        }
    }
    WAITV_BAR(8);
    PS(8);
    if (nt >= 2) ld_wfr();
    WAITV_BAR(0);
    PS(9);
    f32x16 fin = {};
    {
        const lptr po = L + tt * 32768 + lane * 16;
        bf16x8 ofr[8];
#pragma unroll
        for (int k = 0; k < 8; ++k) ofr[k] = lds_b128(po + k * 1024);
        SBAR();
#pragma unroll
        for (int k = 0; k < 32; ++k) {
            fin = MFMA(wfr[k], ofr[k & 7], fin);
            if (k + 8 < 32) ofr[k & 7] = lds_b128(po + (k + 8) * 1024);
            SBAR();
        }
    }
    {
        const lptr ft = L + 98304 + (32 * tt + r31) * 272 + nt * 64 + h * 8;
#pragma unroll
        for (int e = 0; e < 4; ++e) {
            typedef unsigned u32x2 __attribute__((ext_vector_type(2)));
            const u32x2 v = {pk2(fin[4 * e], fin[4 * e + 1]), pk2(fin[4 * e + 2], fin[4 * e + 3])};
            *(LAS u32x2*)(ft + e * 16) = v;
        }
    }
    LBAR();
    {
        const int row = w * 8 + (lane >> 4), ch = lane & 15;
        char* op = out + (size_t)(64 * rb3 + row) * OROW + (b * D_ + 128 * ct) * 2 + ch * 16;
        const lptr fr = L + 98304 + row * 272 + ch * 16;
        const u32x4 v0 = *(const LAS u32x4*)fr, v1 = *(const LAS u32x4*)(fr + 4 * 272);
        if (ch < 8 * nimg) { *(u32x4*)op = v0; *(u32x4*)(op + 4 * OROW) = v1; }
    }
    PS(10);
    if (wg == 0 && w == 0) {
        const u64 t0 = __builtin_amdgcn_s_memrealtime();
        for (;;) {
            bool ok = true;
#pragma unroll
            for (int k = 0; k < 4; ++k) ok &= __hip_atomic_load((const GAS u64*)(ws + WS_FO + (k * 64 + lane) * 128), RLX_AGENT) == epoch;
            if (__all(ok)) break;
            __builtin_amdgcn_s_sleep(1);
            if (__builtin_amdgcn_s_memrealtime() - t0 > 2000000ull) break;
        }
        if (lane == 0) __hip_atomic_store((GAS u64*)(ws + WS_G), epoch, RLX_AGENT);
    }
    PS(11);
}

extern "C" void kernel_launch(void* const* d_in, const int* in_sizes, int n_in, void* d_out, int out_size, void* d_ws, size_t ws_size,
                              hipStream_t stream) {
    (void)hipFuncSetAttribute((const void*)k_fused, hipFuncAttributeMaxDynamicSharedMemorySize, LDS_BYTES);
    k_fused<<<256, 512, LDS_BYTES, stream>>>((const char*)d_in[0], (const char*)d_in[1], (const char*)d_in[2], (const char*)d_in[3],
                                             (const char*)d_in[4], (const char*)d_in[5], (char*)d_out, (char*)d_ws);
}
```

```cpp
#include <hip/hip_runtime.h>
#include <hip/hip_bf16.h>
#include <stdint.h>
#include <type_traits>

typedef __bf16 bf16x8 __attribute__((ext_vector_type(8)));
typedef __bf16 bf16x2 __attribute__((ext_vector_type(2)));
typedef float f32x16 __attribute__((ext_vector_type(16)));
typedef float f32x2 __attribute__((ext_vector_type(2)));
typedef float f32x4 __attribute__((ext_vector_type(4)));
typedef short s16x4 __attribute__((ext_vector_type(4)));
typedef short s16x8 __attribute__((ext_vector_type(8)));
typedef unsigned u32x4 __attribute__((ext_vector_type(4)));
typedef unsigned long long u64;
#define LAS __attribute__((address_space(3)))
#define GAS __attribute__((address_space(1)))
#define DI __device__ __forceinline__
#define MFMA(a, b, c) __builtin_amdgcn_mfma_f32_32x32x16_bf16((a), (b), (c), 0, 0, 0)
#define RLX_AGENT __ATOMIC_RELAXED, __HIP_MEMORY_SCOPE_AGENT

constexpr int S_ = 2048, D_ = 448, NH = 16, DH = 28;
constexpr int XROW = D_ * 2;
constexpr int WROW = 3 * D_ * 2;
constexpr int OROW = 2 * D_ * 2;
constexpr int NTILE = S_ / 32;
constexpr unsigned WS_G = 0;
constexpr unsigned WS_FK = 4096;
constexpr unsigned WS_FV = WS_FK + 256 * 128;
constexpr unsigned WS_FO = WS_FV + 256 * 128;
constexpr unsigned WS_K = 1u << 20;
constexpr unsigned KARR_BYTES = 2u * NH * NTILE * 2048;
constexpr unsigned WS_V = WS_K + KARR_BYTES;
constexpr unsigned WS_O = WS_V + KARR_BYTES;
constexpr unsigned WS_END = WS_O + KARR_BYTES;
constexpr float SCALE_LOG2E = 0.18898223650461362f * 1.4426950408889634f;
constexpr int AUX_SC1 = 16;

DI unsigned pk2(float a, float b) {
    f32x2 p = {a, b};
    return __builtin_bit_cast(unsigned, __builtin_convertvector(p, bf16x2));
}
DI u32x4 pack8u(const f32x16& x, int s) {
    u32x4 w = {pk2(x[8 * s + 0], x[8 * s + 1]), pk2(x[8 * s + 2], x[8 * s + 3]), pk2(x[8 * s + 4], x[8 * s + 5]), pk2(x[8 * s + 6], x[8 * s + 7])};
    return w;
}
DI bf16x8 pack8(const f32x16& x, int s) { return __builtin_bit_cast(bf16x8, pack8u(x, s)); }
DI void glds16(unsigned voff, const void* sbase, unsigned lds_base) {
    unsigned sv;
    asm volatile("s_mov_b32 %0, m0\n\ts_mov_b32 m0, %3\n\ts_nop 0\n\tglobal_load_lds_dwordx4 %1, %2\n\ts_mov_b32 m0, %0"
                 : "=&s"(sv) : "v"(voff), "s"(sbase), "s"(lds_base) : "memory");
}
DI void glds16_nc(unsigned voff, const void* sbase, unsigned lds_base) {
    unsigned sv;
    asm volatile("s_mov_b32 %0, m0\n\ts_mov_b32 m0, %3\n\ts_nop 0\n\tglobal_load_lds_dwordx4 %1, %2\n\ts_mov_b32 m0, %0"
                 : "=&s"(sv) : "v"(voff), "s"(sbase), "s"(lds_base));
}
DI void glds16_sc1(unsigned voff, const void* sbase, unsigned lds_base) {
    unsigned sv;
    asm volatile("s_mov_b32 %0, m0\n\ts_mov_b32 m0, %3\n\ts_nop 0\n\tglobal_load_lds_dwordx4 %1, %2 sc1\n\ts_mov_b32 m0, %0"
                 : "=&s"(sv) : "v"(voff), "s"(sbase), "s"(lds_base) : "memory");
}
typedef LAS char* lptr;
DI bf16x8 lds_b128(lptr p) { return *(const LAS bf16x8*)p; }
DI void lds_w128(lptr p, u32x4 v) { *(LAS u32x4*)p = v; }
DI s16x4 lds_tr(lptr p) { return __builtin_amdgcn_ds_read_tr16_b64_v4i16((LAS s16x4*)p); }
DI bf16x8 cat8(s16x4 lo, s16x4 hi) { s16x8 v = __builtin_shufflevector(lo, hi, 0, 1, 2, 3, 4, 5, 6, 7); return __builtin_bit_cast(bf16x8, v); }
DI bf16x8 tr_frag(lptr a0, lptr a1) { return cat8(lds_tr(a0), lds_tr(a1)); }
#define SBAR() __builtin_amdgcn_sched_barrier(0)

#define WAITV(N) asm volatile("s_waitcnt vmcnt(" #N ")" ::: "memory")
#define WAITV_BAR(N) asm volatile("s_waitcnt vmcnt(" #N ") lgkmcnt(0)\n\ts_barrier" ::: "memory")
#define LBAR() asm volatile("s_waitcnt lgkmcnt(0)\n\ts_barrier" ::: "memory")
template <int N> DI void waitv_bar() {
    if constexpr (N == 2) WAITV_BAR(2); else if constexpr (N == 3) WAITV_BAR(3); else if constexpr (N == 4) WAITV_BAR(4);
    else if constexpr (N == 6) WAITV_BAR(6); else if constexpr (N == 12) WAITV_BAR(12); else LBAR();
}

DI void flag_store(__amdgpu_buffer_rsrc_t rs, unsigned off, int lane, u64 e) {
    typedef unsigned u32x2 __attribute__((ext_vector_type(2)));
    const u32x2 v = {(unsigned)e, (unsigned)(e >> 32)};
    __builtin_amdgcn_raw_buffer_store_b64(v, rs, lane == 0 ? off : 0xF0000000u, 0, AUX_SC1);
}
DI void wait_flags(const char* ws, unsigned off, bool active, u64 e) {
    const GAS u64* p = (const GAS u64*)(ws + off);
    const u64 t0 = __builtin_amdgcn_s_memrealtime();
    for (;;) {
        u64 v = e;
        if (active) v = __hip_atomic_load(p, RLX_AGENT);
        if (__all(v == e)) break;
        __builtin_amdgcn_s_sleep(1);
        if (__builtin_amdgcn_s_memrealtime() - t0 > 2000000ull) break;
    }
    asm volatile("" ::: "memory");
}

constexpr int L_X = 0;
constexpr int L_W = 98304;
constexpr int L_V = 0;
constexpr int L_WO0 = 49152;
constexpr int L_WO1 = 106496;
constexpr int L_K = 114688;
constexpr int LDS_BYTES = 163840;
constexpr int wslot(int n) { return (n >= 12 && n < 18) ? 49152 + (n - 12) * 8192 : L_W + (n % 6) * 8192; }
constexpr int npieces(int n) { return n >= 21 ? 0 : n < 7 ? 3 : 1; }

#define ROT 1

__global__ __launch_bounds__(512, 2) void k_fused(const char* __restrict__ xA, const char* __restrict__ wA, const char* __restrict__ woA,
                                                  const char* __restrict__ xB, const char* __restrict__ wB, const char* __restrict__ woB,
                                                  char* __restrict__ out, char* __restrict__ ws) {
    extern __shared__ __attribute__((aligned(16))) char lds[];
    const u64 epoch = *(const u64*)(ws + WS_G) + 1ull;
    const int xcd = blockIdx.x & 7, idx = blockIdx.x >> 3;
    const int b = xcd >> 2;
    const int r = ((xcd >> 1) & 1) * 8 + (idx >> 2);
    const int g = (xcd & 1) * 4 + (idx & 3);
    const int wg = (b * 16 + r) * 8 + g;
    const int tid = threadIdx.x, lane = tid & 63;
    const int w = __builtin_amdgcn_readfirstlane(tid >> 6);
    const int hh = w >> 2, t = w & 3;
    const char* xb = (b ? xB : xA) + (size_t)r * 128 * XROW;
    const char* wb = (b ? wB : wA) + g * 112;
    const unsigned lbase = (unsigned)(size_t)lds;
    const lptr L = (lptr)lds;
    const __amdgpu_buffer_rsrc_t rs = __builtin_amdgcn_make_buffer_rsrc(ws, 0, WS_END, 0x00020000);

    const unsigned xo0 = (unsigned)((16 * w + (lane >> 3)) * XROW + (((lane & 7) ^ (lane >> 4)) << 4));
    const unsigned xo1 = (unsigned)((16 * w + 8 + (lane >> 3)) * XROW + (((lane & 7) ^ (4 + (lane >> 4))) << 4));
    int wc = (lane & 7) ^ (((lane >> 4) & 1) << 2); wc = wc > 6 ? 6 : wc;
    const unsigned wo0 = (unsigned)((8 * w + (lane >> 3)) * WROW + wc * 16);
    const int rot = ROT == 1 ? (2 * (r & 7) + 3 * (g & 3)) % 7 : ROT == 2 ? ((r + g) & 1) * 3 : ROT == 3 ? ((r + g) % 3) * 2 : ROT == 4 ? ((r & 1) * 2 + (g & 1) * 4) % 7 : 0;

    auto issue_piece = [&](int n, int k) {
        const int pass = n / 7, tau = n % 7;
        int s = tau + rot; s = s >= 7 ? s - 7 : s;
        const unsigned poff = pass == 0 ? 896u : pass == 1 ? 1792u : 0u;
        if (k == 0) glds16(wo0 + s * 64 * WROW + poff, wb, lbase + wslot(n) + w * 1024);
        else glds16((k == 1 ? xo0 : xo1) + s * 128, xb, lbase + L_X + (n % 6) * 16384 + w * 2048 + (k - 1) * 1024);
    };
    auto issue_item = [&](int it) {
#pragma unroll
        for (int k = 0; k < npieces(2 * it); ++k) issue_piece(2 * it, k);
#pragma unroll
        for (int k = 0; k < npieces(2 * it + 1); ++k) issue_piece(2 * it + 1, k);
    };
    auto issue_nth = [&](int it, int p) {
        const int n0 = it <= 2 ? 2 * (it + 3) : it == 3 ? 12 : it <= 6 ? 2 * (it + 4) : 21;
        const int n1 = it <= 2 ? n0 + 2 : it == 3 ? 16 : it <= 6 ? n0 + 2 : 21;
        int c = 0;
#pragma unroll
        for (int n = n0; n < n1; ++n)
#pragma unroll
            for (int k = 0; k < npieces(n); ++k) { if (c == p) issue_piece(n, k); ++c; }
    };
    issue_item(0); issue_item(1); issue_item(2);

    const int r31 = lane & 31, h = lane >> 5;
    const int xrow = 32 * t + r31;
    const unsigned xa = (unsigned)(xrow * 128 + ((h ^ ((xrow >> 1) & 7)) << 4));
    lptr xp0 = L + L_X + xa, xp1 = L + L_X + (xa ^ 32), xp2 = L + L_X + (xa ^ 64), xp3 = L + L_X + (xa ^ 96);
    lptr xq0 = xp0 + 49152, xq1 = xp1 + 49152, xq2 = xp2 + 49152, xq3 = xp3 + 49152;
    asm volatile("" : "+v"(xq0), "+v"(xq1), "+v"(xq2), "+v"(xq3));
    const int blk = (lane >> 4) & 1, q = (lane & 15) >> 2, pp = lane & 3;
    const unsigned wsw = ((q >> 1) & 1) << 6;
    lptr waX = L + 49152 + (unsigned)((8 * h + q) * 128 + ((56 * hh + 32 * blk + 8 * pp) ^ wsw));
    lptr waR = waX + (L_W - 49152);
    asm volatile("" : "+v"(waX), "+v"(waR));

    bf16x8 xf[28];
    f32x16 accK, accV, accQ;
    bf16x8 q0, q1;
    const unsigned gko = (unsigned)(((b * NH + 2 * g + hh) * NTILE + 4 * r + t) * 2048) + lane * 16;
    const lptr ownK = L + L_K + ((8 + t) * 2 + hh) * 2048 + lane * 16, ownV = L + L_V + ((8 + t) * 2 + hh) * 2048 + lane * 16;
    const int j0 = (2 * w) & 7;
    const bool hv0 = r - 2 + (j0 >> 2) >= 0;
    const unsigned hgo = (unsigned)(((b * NH + 2 * g + hh) * NTILE + (hv0 ? 4 * r - 8 + j0 : 0)) * 2048) + lane * 16;
    const unsigned hfo = (unsigned)((hv0 ? wg - 8 * (2 - (j0 >> 2)) : wg) * 128 + (hh * 4 + ((j0 + (lane & 1)) & 3)) * 8);
    u32x4 hk[4];
    u64 kflag = 0;

    auto ldk = [&](int n, int ks, bf16x8& wf) {
        const int pass = n / 7, tau = n % 7;
        const lptr wbase = (wslot(n) >= L_W ? waR + (wslot(n) - L_W) : waX + (wslot(n) - 49152)) + ks * 2048;
        if (pass == 0) {
            if (n % 6 < 3) xf[4 * tau + ks] = lds_b128((ks == 0 ? xp0 : ks == 1 ? xp1 : ks == 2 ? xp2 : xp3) + (n % 6) * 16384);
            else xf[4 * tau + ks] = lds_b128((ks == 0 ? xq0 : ks == 1 ? xq1 : ks == 2 ? xq2 : xq3) + (n % 6 - 3) * 16384);
        }
        wf = tr_frag(wbase, wbase + 512);
    };
    auto mma = [&](int n, int ks, const bf16x8& wf) {
        const int pass = n / 7, tau = n % 7;
        const bool first = tau == 0 && ks == 0;
        const f32x16 zero = {};
        if (pass == 0) accK = MFMA(wf, xf[4 * tau + ks], first ? zero : accK);
        else if (pass == 1) accV = MFMA(xf[4 * tau + ks], wf, first ? zero : accV);
        else accQ = MFMA(wf, xf[4 * tau + ks], first ? zero : accQ);
    };
    u32x4 e0, e1;
    const int ct = g & 3, rb3 = 2 * r + (g >> 2);
    const int nimg = ct == 3 ? 1 : 2;
    const char* wob = (b ? woB : woA) + ct * 256;
    const unsigned wo3 = (unsigned)((lane >> 3) * XROW + (((lane & 7) ^ (((lane >> 4) & 1) << 2)) << 4));
#define WOP(i) glds16_nc(wo3 + (6 * w + (i)) * 8 * XROW, wob, lbase + L_WO0 + (6 * w + (i)) * 1024)
    auto hook = [&](int it, int k) {
        if (it == 3 && k == 5) {
            e0 = pack8u(accK, 0); e1 = pack8u(accK, 1);
            e1[2] = h ? 0u : e1[2]; e1[3] = h ? 0u : e1[3];
        }
        if (it == 3 && k == 6) { lds_w128(ownK, e0); lds_w128(ownK + 1024, e1); }
        if (it == 3 && k == 7) {
            __builtin_amdgcn_raw_buffer_store_b128(e0, rs, WS_K + gko, 0, AUX_SC1);
            __builtin_amdgcn_raw_buffer_store_b128(e1, rs, WS_K + gko + 1024, 0, AUX_SC1);
        }
        if (it == 7 && k == 0)
            flag_store(rs, WS_FK + wg * 128 + w * 8, lane, epoch);
        if (it == 7 && k == 1) {
            e0 = pack8u(accV, 0); e1 = pack8u(accV, 1);
            unsigned vv = (r31 == DH) ? 0x3F803F80u : 0u, keep = r31 < DH ? ~0u : 0u;
            asm volatile("" : "+v"(vv), "+v"(keep));
#pragma unroll
            for (int i = 0; i < 4; ++i) { e0[i] = (e0[i] & keep) | vv; e1[i] = (e1[i] & keep) | vv; }
        }
        if (it == 7 && k == 2) { lds_w128(ownV, e0); lds_w128(ownV + 1024, e1); }
        if (it == 7 && k == 3) {
            __builtin_amdgcn_raw_buffer_store_b128(e0, rs, WS_V + gko, 0, AUX_SC1);
            __builtin_amdgcn_raw_buffer_store_b128(e1, rs, WS_V + gko + 1024, 0, AUX_SC1);
        }
        if (it == 8 && (k & 1) == 1 && k >= 3) WOP((k - 3) >> 1);
        if (it == 9 && (k & 1) == 1 && k >= 3) WOP(3 + ((k - 3) >> 1));
        if (it == 9 && k == 0) kflag = __hip_atomic_load((const GAS u64*)(ws + WS_FK + hfo), RLX_AGENT);
    };

    bf16x8 wf[2][8];
    waitv_bar<12>();
#pragma unroll
    for (int k = 0; k < 8; ++k) ldk(k >> 2, k & 3, wf[0][k]);
    auto run_item = [&](auto IT) {
        constexpr int it = decltype(IT)::value;
        constexpr int cnt = it == 0 ? 6 : it == 1 ? 4 : it <= 3 ? 2 : it <= 6 ? 6 : it == 7 ? 3 : it == 8 ? 4 : it == 9 ? 6 : -1;
        waitv_bar<cnt>();
        if (it == 10) {
            if (hv0) {
                if (!__all(lane >= 2 || kflag == epoch)) wait_flags(ws, WS_FK + hfo, lane < 2, epoch);
                asm volatile("" ::: "memory");
#pragma unroll
                for (int k = 0; k < 4; ++k) hk[k] = __builtin_amdgcn_raw_buffer_load_b128(rs, WS_K + hgo + k * 1024, 0, AUX_SC1);
            } else {
#pragma unroll
                for (int k = 0; k < 4; ++k) hk[k] = (u32x4){0u, 0u, 0u, 0u};
            }
        }
        SBAR();
#pragma unroll
        for (int k = 0; k < 8; ++k) {
            const int n = 2 * it + (k >> 2);
            if (n < 21) mma(n, k & 3, wf[it & 1][k]);
            if (2 * it + 2 + (k >> 2) < 21) ldk(2 * it + 2 + (k >> 2), k & 3, wf[(it + 1) & 1][k]);
            {
                constexpr int np = it == 0 ? 4 : it <= 2 ? 2 : it == 3 ? 4 : it <= 5 ? 2 : it == 6 ? 1 : 0;
                if (np == 4) { if ((k & 1) == 0 && !(it == 3)) issue_nth(it, k >> 1); if (it == 3 && k < 4) issue_nth(it, k); }
                else if (np == 2) { if ((k & 3) == 1) issue_nth(it, k >> 2); }
                else if (np == 1) { if (k == 1) issue_nth(it, 0); }
            }
            hook(it, k);
            SBAR();
        }
    };
    run_item(std::integral_constant<int, 0>{}); run_item(std::integral_constant<int, 1>{}); run_item(std::integral_constant<int, 2>{});
    run_item(std::integral_constant<int, 3>{}); run_item(std::integral_constant<int, 4>{}); run_item(std::integral_constant<int, 5>{});
    run_item(std::integral_constant<int, 6>{}); run_item(std::integral_constant<int, 7>{}); run_item(std::integral_constant<int, 8>{});
    run_item(std::integral_constant<int, 9>{}); run_item(std::integral_constant<int, 10>{});
#pragma unroll
    for (int i = 0; i < 16; ++i) accQ[i] *= SCALE_LOG2E;
    q0 = pack8(accQ, 0);
    { u32x4 e = pack8u(accQ, 1); e[2] = h ? 0u : e[2]; e[3] = h ? 0u : e[3]; q1 = __builtin_bit_cast(bf16x8, e); }

    if (hv0) WAITV(11); else WAITV(7);
    flag_store(rs, WS_FV + wg * 128 + w * 8, lane, epoch);
    LBAR();
    {
        const lptr lo = L + L_K + (j0 * 2 + hh) * 2048 + lane * 16;
        lds_w128(lo, hk[0]); lds_w128(lo + 1024, hk[1]); lds_w128(lo + 4096, hk[2]); lds_w128(lo + 4096 + 1024, hk[3]);
    }
    LBAR();
    glds16_nc(wo3 + (48 + w) * 8 * XROW, wob, lbase + L_WO0 + (48 + w) * 1024);
    const float NEG = -__builtin_inff();
    const lptr kb = L + L_K + (t * 2 + hh) * 2048 + lane * 16;
    const lptr vb = L + L_V + (t * 2 + hh) * 2048 + lane * 16;
    f32x16 o = {};
    auto attn = [&](auto EDGE_) {
        constexpr bool EDGE = decltype(EDGE_)::value;
        bf16x8 kf0[9], kf1[9];
#pragma unroll
        for (int j = 0; j < 9; ++j) { kf0[j] = lds_b128(kb + j * 4096); kf1[j] = lds_b128(kb + j * 4096 + 1024); }
        const f32x16 zero = {};
#define S_TILE(dst, j) do { dst = MFMA(kf0[j], q0, zero); dst = MFMA(kf1[j], q1, dst); } while (0)
#define MX_TILE(src, j) do { if (!(EDGE && 4 * r - 8 + t + (j) < 0)) { _Pragma("unroll") for (int i_ = 0; i_ < 16; i_ += 4) { m0 = __builtin_fmaxf(__builtin_fmaxf(m0, src[i_]), src[i_ + 1]); m1 = __builtin_fmaxf(__builtin_fmaxf(m1, src[i_ + 2]), src[i_ + 3]); } } } while (0)
        f32x16 s0, mg, y0, y1, y2;
        float m0 = NEG, m1 = NEG;
        S_TILE(s0, 0); S_TILE(mg, 8); S_TILE(y0, 1); S_TILE(y1, 2);
        SBAR();
        S_TILE(y2, 3);
        if (EDGE && 4 * r - 8 + t < 0) {
#pragma unroll
            for (int i = 0; i < 16; ++i) s0[i] = NEG;
        }
#pragma unroll
        for (int i = 0; i < 16; ++i) { const int c = (i & 3) + 8 * (i >> 2) + 4 * h; mg[i] = c > r31 ? s0[i] : mg[i]; }
#pragma unroll
        for (int i = 0; i < 16; i += 4) { m0 = __builtin_fmaxf(__builtin_fmaxf(m0, mg[i]), mg[i + 1]); m1 = __builtin_fmaxf(__builtin_fmaxf(m1, mg[i + 2]), mg[i + 3]); }
        SBAR();
        MX_TILE(y0, 1); SBAR();
        S_TILE(y0, 4); MX_TILE(y1, 2); SBAR();
        S_TILE(y1, 5); MX_TILE(y2, 3); SBAR();
        S_TILE(y2, 6); MX_TILE(y0, 4); SBAR();
        S_TILE(y0, 7); MX_TILE(y1, 5); SBAR();
        MX_TILE(y2, 6); SBAR();
        MX_TILE(y0, 7);
        float m = __builtin_fmaxf(m0, m1);
        {
            auto rr = __builtin_amdgcn_permlane32_swap(__float_as_uint(m), __float_as_uint(m), false, false);
            m = __builtin_fmaxf(__uint_as_float(rr[0]), __uint_as_float(rr[1]));
        }
        f32x16 cm;
#pragma unroll
        for (int i = 0; i < 16; ++i) cm[i] = -m;
#define SM_TILE(dst, j) do { dst = MFMA(kf0[j], q0, cm); dst = MFMA(kf1[j], q1, dst); } while (0)
        SM_TILE(y0, 7); SM_TILE(y1, 6);
        const u64 vflag = __hip_atomic_load((const GAS u64*)(ws + WS_FV + hfo), RLX_AGENT);
        bf16x8 pa[9], pb[9];
#define EX_TILE(src, j) do { if (EDGE && 4 * r - 8 + t + (j) < 0) { _Pragma("unroll") for (int i_ = 0; i_ < 16; ++i_) src[i_] = 0.f; } else { _Pragma("unroll") for (int i_ = 0; i_ < 16; ++i_) src[i_] = __builtin_amdgcn_exp2f(src[i_]); } \
        pa[j] = pack8(src, 0); pb[j] = pack8(src, 1); } while (0)
#define PV_TILE(j) do { const bf16x8 v0_ = lds_b128(vb + (j) * 4096), v1_ = lds_b128(vb + (j) * 4096 + 1024); o = MFMA(v0_, pa[j], o); o = MFMA(v1_, pb[j], o); } while (0)
        {
#pragma unroll
            for (int i = 0; i < 16; ++i) mg[i] = __builtin_amdgcn_exp2f(mg[i] - m);
#pragma unroll
            for (int i = 0; i < 16; ++i) {
                const int c = (i & 3) + 8 * (i >> 2) + 4 * h;
                s0[i] = c > r31 ? mg[i] : 0.f;
                mg[i] = c > r31 ? 0.f : mg[i];
            }
            pa[8] = pack8(mg, 0); pb[8] = pack8(mg, 1); pa[0] = pack8(s0, 0); pb[0] = pack8(s0, 1);
        }
        SBAR();
        SM_TILE(y2, 5); PV_TILE(8);
        EX_TILE(y0, 7); SBAR();
        SM_TILE(y0, 4);
        EX_TILE(y1, 6); SBAR();
        u32x4 hv[4];
        if (hv0) {
            if (!__all(lane >= 2 || vflag == epoch)) wait_flags(ws, WS_FV + hfo, lane < 2, epoch);
            asm volatile("" ::: "memory");
#pragma unroll
            for (int k = 0; k < 4; ++k) hv[k] = __builtin_amdgcn_raw_buffer_load_b128(rs, WS_V + hgo + k * 1024, 0, AUX_SC1);
        } else {
#pragma unroll
            for (int k = 0; k < 4; ++k) hv[k] = (u32x4){0u, 0u, 0u, 0u};
        }
        SM_TILE(y1, 3);
        EX_TILE(y2, 5); SBAR();
        SM_TILE(y2, 2);
        EX_TILE(y0, 4); SBAR();
        SM_TILE(y0, 1);
        EX_TILE(y1, 3);
        {
            const lptr lo = L + L_V + (j0 * 2 + hh) * 2048 + lane * 16;
            lds_w128(lo, hv[0]); lds_w128(lo + 1024, hv[1]); lds_w128(lo + 4096, hv[2]); lds_w128(lo + 4096 + 1024, hv[3]);
        }
        LBAR();
        PV_TILE(7); PV_TILE(6); PV_TILE(5);
        EX_TILE(y2, 2); SBAR();
        PV_TILE(4); PV_TILE(0); PV_TILE(3);
        EX_TILE(y0, 1); SBAR();
        PV_TILE(2); PV_TILE(1);
    };
    if (__builtin_expect(r >= 2, 1)) attn(std::false_type{}); else attn(std::true_type{});
    {
        float l = o[12];
        auto rr = __builtin_amdgcn_permlane32_swap(__float_as_uint(l), __float_as_uint(l), false, false);
        l = __uint_as_float(rr[1]);
        const float inv = 1.0f / l;
#pragma unroll
        for (int i = 0; i < 16; ++i) o[i] *= inv;
        if (h == 1) {
#pragma unroll
            for (int i = 12; i < 16; ++i) o[i] = 0.f;
        }
        const unsigned go = WS_O + (unsigned)(((b * NTILE + 4 * r + t) * NH + 2 * g + hh) * 2048) + lane * 16;
        __builtin_amdgcn_raw_buffer_store_b128(pack8u(o, 0), rs, go, 0, AUX_SC1);
        __builtin_amdgcn_raw_buffer_store_b128(pack8u(o, 1), rs, go + 1024, 0, AUX_SC1);
        if (nimg == 2) {
#pragma unroll
            for (int i = 0; i < 7; ++i) glds16_nc(wo3 + 128 + (7 * w + i) * 8 * XROW, wob, lbase + L_WO1 + (7 * w + i) * 1024);
            WAITV(7);
        } else WAITV(0);
        flag_store(rs, WS_FO + wg * 128 + w * 8, lane, epoch);
    }

    const int tt = w & 1, nt = w >> 1;
    bf16x8 wfr[32];
    const int row0 = 4 * h + q;
    const lptr pw = L + L_WO0 + (nt >> 1) * (L_WO1 - L_WO0) + row0 * 128 + ((((nt & 1) << 6) + 32 * blk + 8 * pp) ^ wsw);
    const lptr pwl = pw + (h ? (444 - 15 * DH - 24 - 4) * 128 : 0);
    auto ld_wfr = [&]() {
#pragma unroll
        for (int hd = 0; hd < 16; ++hd)
#pragma unroll
            for (int s = 0; s < 2; ++s) {
                const int ro = (hd * DH + 16 * s) * 128;
                if (hd == 15 && s == 1) wfr[2 * hd + s] = tr_frag(pw + ro, pwl + ro + 1024);
                else wfr[2 * hd + s] = tr_frag(pw + ro, pw + ro + 1024);
            }
    };
    LBAR();
    if (nt < 2) ld_wfr();
    LBAR();
    wait_flags(ws, WS_FO + (unsigned)(((b * 16 + r) * 8 + w) * 128 + ((lane & 1) * 4 + 2 * (g >> 2) + ((lane >> 1) & 1)) * 8), lane < 4, epoch);
    {
        const char* ob = ws + WS_O;
        const unsigned go = (unsigned)(((b * NTILE + 2 * rb3) * NH + 2 * w) * 2048) + lane * 16;
#pragma unroll
        for (int i = 0; i < 4; ++i) {
            glds16_sc1(go + i * 1024, ob, lbase + w * 4096 + i * 1024);
            glds16_sc1(go + NH * 2048 + i * 1024, ob, lbase + 32768 + w * 4096 + i * 1024);
        }
    }
    WAITV_BAR(8);
    if (nt >= 2) ld_wfr();
    WAITV_BAR(0);
    f32x16 fin = {};
    {
        const lptr po = L + tt * 32768 + lane * 16;
        bf16x8 ofr[8];
#pragma unroll
        for (int k = 0; k < 8; ++k) ofr[k] = lds_b128(po + k * 1024);
        SBAR();
#pragma unroll
        for (int k = 0; k < 32; ++k) {
            fin = MFMA(wfr[k], ofr[k & 7], fin);
            if (k + 8 < 32) ofr[k & 7] = lds_b128(po + (k + 8) * 1024);
            SBAR();
        }
    }
    {
        const lptr ft = L + 98304 + (32 * tt + r31) * 272 + nt * 64 + h * 8;
#pragma unroll
        for (int e = 0; e < 4; ++e) {
            typedef unsigned u32x2 __attribute__((ext_vector_type(2)));
            const u32x2 v = {pk2(fin[4 * e], fin[4 * e + 1]), pk2(fin[4 * e + 2], fin[4 * e + 3])};
            *(LAS u32x2*)(ft + e * 16) = v;
        }
    }
    LBAR();
    {
        const int row = w * 8 + (lane >> 4), ch = lane & 15;
        char* op = out + (size_t)(64 * rb3 + row) * OROW + (b * D_ + 128 * ct) * 2 + ch * 16;
        const lptr fr = L + 98304 + row * 272 + ch * 16;
        const u32x4 v0 = *(const LAS u32x4*)fr, v1 = *(const LAS u32x4*)(fr + 4 * 272);
        if (ch < 8 * nimg) { *(u32x4*)op = v0; *(u32x4*)(op + 4 * OROW) = v1; }
    }
    if (wg == 0 && w == 0) {
        const u64 t0 = __builtin_amdgcn_s_memrealtime();
        for (;;) {
            bool ok = true;
#pragma unroll
            for (int k = 0; k < 4; ++k) ok &= __hip_atomic_load((const GAS u64*)(ws + WS_FO + (k * 64 + lane) * 128), RLX_AGENT) == epoch;
            if (__all(ok)) break;
            __builtin_amdgcn_s_sleep(1);
            if (__builtin_amdgcn_s_memrealtime() - t0 > 2000000ull) break;
        }
        if (lane == 0) __hip_atomic_store((GAS u64*)(ws + WS_G), epoch, RLX_AGENT);
    }
}

extern "C" void kernel_launch(void* const* d_in, const int* in_sizes, int n_in, void* d_out, int out_size, void* d_ws, size_t ws_size,
                              hipStream_t stream) {
    (void)hipFuncSetAttribute((const void*)k_fused, hipFuncAttributeMaxDynamicSharedMemorySize, LDS_BYTES);
    k_fused<<<256, 512, LDS_BYTES, stream>>>((const char*)d_in[0], (const char*)d_in[1], (const char*)d_in[2], (const char*)d_in[3],
                                             (const char*)d_in[4], (const char*)d_in[5], (char*)d_out, (char*)d_ws);
}
```

```cpp
#include <hip/hip_runtime.h>
#include <hip/hip_bf16.h>
#include <stdint.h>
#include <type_traits>

typedef __bf16 bf16x8 __attribute__((ext_vector_type(8)));
typedef __bf16 bf16x2 __attribute__((ext_vector_type(2)));
typedef float f32x16 __attribute__((ext_vector_type(16)));
typedef float f32x2 __attribute__((ext_vector_type(2)));
typedef float f32x4 __attribute__((ext_vector_type(4)));
typedef short s16x4 __attribute__((ext_vector_type(4)));
typedef short s16x8 __attribute__((ext_vector_type(8)));
typedef unsigned u32x4 __attribute__((ext_vector_type(4)));
typedef unsigned long long u64;
#define LAS __attribute__((address_space(3)))
#define GAS __attribute__((address_space(1)))
#define DI __device__ __forceinline__
#define MFMA(a, b, c) __builtin_amdgcn_mfma_f32_32x32x16_bf16((a), (b), (c), 0, 0, 0)
#define RLX_AGENT __ATOMIC_RELAXED, __HIP_MEMORY_SCOPE_AGENT

constexpr int S_ = 2048, D_ = 448, NH = 16, DH = 28;
constexpr int XROW = D_ * 2;
constexpr int WROW = 3 * D_ * 2;
constexpr int OROW = 2 * D_ * 2;
constexpr int NTILE = S_ / 32;
constexpr unsigned WS_G = 0;
constexpr unsigned WS_FK = 4096;
constexpr unsigned WS_FV = WS_FK + 256 * 128;
constexpr unsigned WS_FO = WS_FV + 256 * 128;
constexpr unsigned WS_K = 1u << 20;
constexpr unsigned KARR_BYTES = 2u * NH * NTILE * 2048;
constexpr unsigned WS_V = WS_K + KARR_BYTES;
constexpr unsigned WS_O = WS_V + KARR_BYTES;
constexpr unsigned WS_END = WS_O + KARR_BYTES;
constexpr float SCALE_LOG2E = 0.18898223650461362f * 1.4426950408889634f;
constexpr int AUX_SC1 = 16;

DI unsigned pk2(float a, float b) {
    f32x2 p = {a, b};
    return __builtin_bit_cast(unsigned, __builtin_convertvector(p, bf16x2));
}
DI u32x4 pack8u(const f32x16& x, int s) {
    u32x4 w = {pk2(x[8 * s + 0], x[8 * s + 1]), pk2(x[8 * s + 2], x[8 * s + 3]), pk2(x[8 * s + 4], x[8 * s + 5]), pk2(x[8 * s + 6], x[8 * s + 7])};
    return w;
}
DI bf16x8 pack8(const f32x16& x, int s) { return __builtin_bit_cast(bf16x8, pack8u(x, s)); }
DI void glds16(unsigned voff, const void* sbase, unsigned lds_base) {
    unsigned sv;
    asm volatile("s_mov_b32 %0, m0\n\ts_mov_b32 m0, %3\n\ts_nop 0\n\tglobal_load_lds_dwordx4 %1, %2\n\ts_mov_b32 m0, %0"
                 : "=&s"(sv) : "v"(voff), "s"(sbase), "s"(lds_base) : "memory");
}
DI void glds16_nc(unsigned voff, const void* sbase, unsigned lds_base) {
    unsigned sv;
    asm volatile("s_mov_b32 %0, m0\n\ts_mov_b32 m0, %3\n\ts_nop 0\n\tglobal_load_lds_dwordx4 %1, %2\n\ts_mov_b32 m0, %0"
                 : "=&s"(sv) : "v"(voff), "s"(sbase), "s"(lds_base));
}
DI void glds16_sc1(unsigned voff, const void* sbase, unsigned lds_base) {
    unsigned sv;
    asm volatile("s_mov_b32 %0, m0\n\ts_mov_b32 m0, %3\n\ts_nop 0\n\tglobal_load_lds_dwordx4 %1, %2 sc1\n\ts_mov_b32 m0, %0"
                 : "=&s"(sv) : "v"(voff), "s"(sbase), "s"(lds_base) : "memory");
}
typedef LAS char* lptr;
DI bf16x8 lds_b128(lptr p) { return *(const LAS bf16x8*)p; }
DI void lds_w128(lptr p, u32x4 v) { *(LAS u32x4*)p = v; }
DI s16x4 lds_tr(lptr p) { return __builtin_amdgcn_ds_read_tr16_b64_v4i16((LAS s16x4*)p); }
DI bf16x8 cat8(s16x4 lo, s16x4 hi) { s16x8 v = __builtin_shufflevector(lo, hi, 0, 1, 2, 3, 4, 5, 6, 7); return __builtin_bit_cast(bf16x8, v); }
DI bf16x8 tr_frag(lptr a0, lptr a1) { return cat8(lds_tr(a0), lds_tr(a1)); }
#define SBAR() __builtin_amdgcn_sched_barrier(0)

#define WAITV(N) asm volatile("s_waitcnt vmcnt(" #N ")" ::: "memory")
#define WAITV_BAR(N) asm volatile("s_waitcnt vmcnt(" #N ") lgkmcnt(0)\n\ts_barrier" ::: "memory")
#define LBAR() asm volatile("s_waitcnt lgkmcnt(0)\n\ts_barrier" ::: "memory")
template <int N> DI void waitv_bar() {
    if constexpr (N == 2) WAITV_BAR(2); else if constexpr (N == 3) WAITV_BAR(3); else if constexpr (N == 4) WAITV_BAR(4);
    else if constexpr (N == 6) WAITV_BAR(6); else if constexpr (N == 12) WAITV_BAR(12); else LBAR();
}

DI void flag_store(__amdgpu_buffer_rsrc_t rs, unsigned off, int lane, u64 e) {
    typedef unsigned u32x2 __attribute__((ext_vector_type(2)));
    const u32x2 v = {(unsigned)e, (unsigned)(e >> 32)};
    __builtin_amdgcn_raw_buffer_store_b64(v, rs, lane == 0 ? off : 0xF0000000u, 0, AUX_SC1);
}
DI void wait_flags(const char* ws, unsigned off, bool active, u64 e) {
    const GAS u64* p = (const GAS u64*)(ws + off);
    const u64 t0 = __builtin_amdgcn_s_memrealtime();
    for (;;) {
        u64 v = e;
        if (active) v = __hip_atomic_load(p, RLX_AGENT);
        if (__all(v == e)) break;
        __builtin_amdgcn_s_sleep(1);
        if (__builtin_amdgcn_s_memrealtime() - t0 > 2000000ull) break;
    }
    asm volatile("" ::: "memory");
}

constexpr int L_X = 0;
constexpr int L_W = 98304;
constexpr int L_V = 0;
constexpr int L_WO0 = 49152;
constexpr int L_WO1 = 106496;
constexpr int L_K = 114688;
constexpr int LDS_BYTES = 163840;
constexpr int wslot(int n) { return (n >= 12 && n < 18) ? 49152 + (n - 12) * 8192 : L_W + (n % 6) * 8192; }
constexpr int npieces(int n) { return n >= 21 ? 0 : n < 7 ? 3 : 1; }

#define ROT 1

__global__ __launch_bounds__(512, 2) void k_fused(const char* __restrict__ xA, const char* __restrict__ wA, const char* __restrict__ woA,
                                                  const char* __restrict__ xB, const char* __restrict__ wB, const char* __restrict__ woB,
                                                  char* __restrict__ out, char* __restrict__ ws) {
    extern __shared__ __attribute__((aligned(16))) char lds[];
    const u64 epoch = *(const u64*)(ws + WS_G) + 1ull;
    const int xcd = blockIdx.x & 7, idx = blockIdx.x >> 3;
    const int b = xcd >> 2;
    const int r = ((xcd >> 1) & 1) * 8 + (idx >> 2);
    const int g = (xcd & 1) * 4 + (idx & 3);
    const int wg = (b * 16 + r) * 8 + g;
    const int tid = threadIdx.x, lane = tid & 63;
    const int w = __builtin_amdgcn_readfirstlane(tid >> 6);
    const int hh = w >> 2, t = w & 3;
    const char* xb = (b ? xB : xA) + (size_t)r * 128 * XROW;
    const char* wb = (b ? wB : wA) + g * 112;
    const unsigned lbase = (unsigned)(size_t)lds;
    const lptr L = (lptr)lds;
    const __amdgpu_buffer_rsrc_t rs = __builtin_amdgcn_make_buffer_rsrc(ws, 0, WS_END, 0x00020000);

    const unsigned xo0 = (unsigned)((16 * w + (lane >> 3)) * XROW + (((lane & 7) ^ (lane >> 4)) << 4));
    const unsigned xo1 = (unsigned)((16 * w + 8 + (lane >> 3)) * XROW + (((lane & 7) ^ (4 + (lane >> 4))) << 4));
    int wc = (lane & 7) ^ (((lane >> 4) & 1) << 2); wc = wc > 6 ? 6 : wc;
    const unsigned wo0 = (unsigned)((8 * w + (lane >> 3)) * WROW + wc * 16);
    const int rot = ROT == 1 ? (2 * (r & 7) + 3 * (g & 3)) % 7 : ROT == 2 ? ((r + g) & 1) * 3 : ROT == 3 ? ((r + g) % 3) * 2 : ROT == 4 ? ((r & 1) * 2 + (g & 1) * 4) % 7 : 0;

    auto issue_piece = [&](int n, int k) {
        const int pass = n / 7, tau = n % 7;
        int s = tau + rot; s = s >= 7 ? s - 7 : s;
        const unsigned poff = pass == 0 ? 896u : pass == 1 ? 1792u : 0u;
        if (k == 0) glds16(wo0 + s * 64 * WROW + poff, wb, lbase + wslot(n) + w * 1024);
        else glds16((k == 1 ? xo0 : xo1) + s * 128, xb, lbase + L_X + (n % 6) * 16384 + w * 2048 + (k - 1) * 1024);
    };
    auto issue_item = [&](int it) {
#pragma unroll
        for (int k = 0; k < npieces(2 * it); ++k) issue_piece(2 * it, k);
#pragma unroll
        for (int k = 0; k < npieces(2 * it + 1); ++k) issue_piece(2 * it + 1, k);
    };
    auto issue_nth = [&](int it, int p) {
        const int n0 = it <= 2 ? 2 * (it + 3) : it == 3 ? 12 : it <= 6 ? 2 * (it + 4) : 21;
        const int n1 = it <= 2 ? n0 + 2 : it == 3 ? 16 : it <= 6 ? n0 + 2 : 21;
        int c = 0;
#pragma unroll
        for (int n = n0; n < n1; ++n)
#pragma unroll
            for (int k = 0; k < npieces(n); ++k) { if (c == p) issue_piece(n, k); ++c; }
    };
    issue_item(0); issue_item(1); issue_item(2);

    const int r31 = lane & 31, h = lane >> 5;
    const int xrow = 32 * t + r31;
    const unsigned xa = (unsigned)(xrow * 128 + ((h ^ ((xrow >> 1) & 7)) << 4));
    lptr xp0 = L + L_X + xa, xp1 = L + L_X + (xa ^ 32), xp2 = L + L_X + (xa ^ 64), xp3 = L + L_X + (xa ^ 96);
    lptr xq0 = xp0 + 49152, xq1 = xp1 + 49152, xq2 = xp2 + 49152, xq3 = xp3 + 49152;
    asm volatile("" : "+v"(xq0), "+v"(xq1), "+v"(xq2), "+v"(xq3));
    const int blk = (lane >> 4) & 1, q = (lane & 15) >> 2, pp = lane & 3;
    const unsigned wsw = ((q >> 1) & 1) << 6;
    lptr waX = L + 49152 + (unsigned)((8 * h + q) * 128 + ((56 * hh + 32 * blk + 8 * pp) ^ wsw));
    lptr waR = waX + (L_W - 49152);
    asm volatile("" : "+v"(waX), "+v"(waR));

    bf16x8 xf[28];
    f32x16 accK, accV, accQ;
    bf16x8 q0, q1;
    const unsigned gko = (unsigned)(((b * NH + 2 * g + hh) * NTILE + 4 * r + t) * 2048) + lane * 16;
    const lptr ownK = L + L_K + ((8 + t) * 2 + hh) * 2048 + lane * 16, ownV = L + L_V + ((8 + t) * 2 + hh) * 2048 + lane * 16;
    const int j0 = (2 * w) & 7;
    const bool hv0 = r - 2 + (j0 >> 2) >= 0;
    const unsigned hgo = (unsigned)(((b * NH + 2 * g + hh) * NTILE + (hv0 ? 4 * r - 8 + j0 : 0)) * 2048) + lane * 16;
    const unsigned hfo = (unsigned)((hv0 ? wg - 8 * (2 - (j0 >> 2)) : wg) * 128 + (hh * 4 + ((j0 + (lane & 1)) & 3)) * 8);
    u32x4 hk[4];
    u64 kflag = 0;

    auto ldk = [&](int n, int ks, bf16x8& wf) {
        const int pass = n / 7, tau = n % 7;
        const lptr wbase = (wslot(n) >= L_W ? waR + (wslot(n) - L_W) : waX + (wslot(n) - 49152)) + ks * 2048;
        if (pass == 0) {
            if (n % 6 < 3) xf[4 * tau + ks] = lds_b128((ks == 0 ? xp0 : ks == 1 ? xp1 : ks == 2 ? xp2 : xp3) + (n % 6) * 16384);
            else xf[4 * tau + ks] = lds_b128((ks == 0 ? xq0 : ks == 1 ? xq1 : ks == 2 ? xq2 : xq3) + (n % 6 - 3) * 16384);
        }
        wf = tr_frag(wbase, wbase + 512);
    };
    auto mma = [&](int n, int ks, const bf16x8& wf) {
        const int pass = n / 7, tau = n % 7;
        const bool first = tau == 0 && ks == 0;
        const f32x16 zero = {};
        if (pass == 0) accK = MFMA(wf, xf[4 * tau + ks], first ? zero : accK);
        else if (pass == 1) accV = MFMA(xf[4 * tau + ks], wf, first ? zero : accV);
        else accQ = MFMA(wf, xf[4 * tau + ks], first ? zero : accQ);
    };
    u32x4 e0, e1;
    const int ct = g & 3, rb3 = 2 * r + (g >> 2);
    const int nimg = ct == 3 ? 1 : 2;
    const char* wob = (b ? woB : woA) + ct * 256;
    const unsigned wo3 = (unsigned)((lane >> 3) * XROW + (((lane & 7) ^ (((lane >> 4) & 1) << 2)) << 4));
#define WOP(i) glds16_nc(wo3 + (6 * w + (i)) * 8 * XROW, wob, lbase + L_WO0 + (6 * w + (i)) * 1024)
    auto hook = [&](int it, int k) {
        if (it == 3 && k == 5) {
            e0 = pack8u(accK, 0); e1 = pack8u(accK, 1);
            e1[2] = h ? 0u : e1[2]; e1[3] = h ? 0u : e1[3];
        }
        if (it == 3 && k == 6) { lds_w128(ownK, e0); lds_w128(ownK + 1024, e1); }
        if (it == 3 && k == 7) {
            __builtin_amdgcn_raw_buffer_store_b128(e0, rs, WS_K + gko, 0, AUX_SC1);
            __builtin_amdgcn_raw_buffer_store_b128(e1, rs, WS_K + gko + 1024, 0, AUX_SC1);
        }
        if (it == 7 && k == 0)
            flag_store(rs, WS_FK + wg * 128 + w * 8, lane, epoch);
        if (it == 7 && k == 1) {
            e0 = pack8u(accV, 0); e1 = pack8u(accV, 1);
            unsigned vv = (r31 == DH) ? 0x3F803F80u : 0u, keep = r31 < DH ? ~0u : 0u;
            asm volatile("" : "+v"(vv), "+v"(keep));
#pragma unroll
            for (int i = 0; i < 4; ++i) { e0[i] = (e0[i] & keep) | vv; e1[i] = (e1[i] & keep) | vv; }
        }
        if (it == 7 && k == 2) { lds_w128(ownV, e0); lds_w128(ownV + 1024, e1); }
        if (it == 7 && k == 3) {
            __builtin_amdgcn_raw_buffer_store_b128(e0, rs, WS_V + gko, 0, AUX_SC1);
            __builtin_amdgcn_raw_buffer_store_b128(e1, rs, WS_V + gko + 1024, 0, AUX_SC1);
        }
        if (it == 8 && (k & 1) == 1 && k >= 3) WOP((k - 3) >> 1);
        if (it == 9 && (k & 1) == 1 && k >= 3) WOP(3 + ((k - 3) >> 1));
        if (it == 9 && k == 0) kflag = __hip_atomic_load((const GAS u64*)(ws + WS_FK + hfo), RLX_AGENT);
    };

    bf16x8 wf[2][8];
    waitv_bar<12>();
#pragma unroll
    for (int k = 0; k < 8; ++k) ldk(k >> 2, k & 3, wf[0][k]);
    auto run_item = [&](auto IT) {
        constexpr int it = decltype(IT)::value;
        constexpr int cnt = it == 0 ? 6 : it == 1 ? 4 : it <= 3 ? 2 : it <= 6 ? 6 : it == 7 ? 3 : it == 8 ? 4 : it == 9 ? 6 : -1;
        waitv_bar<cnt>();
        if (it == 10) {
            if (hv0) {
                if (!__all(lane >= 2 || kflag == epoch)) wait_flags(ws, WS_FK + hfo, lane < 2, epoch);
                asm volatile("" ::: "memory");
#pragma unroll
                for (int k = 0; k < 4; ++k) hk[k] = __builtin_amdgcn_raw_buffer_load_b128(rs, WS_K + hgo + k * 1024, 0, AUX_SC1);
            } else {
#pragma unroll
                for (int k = 0; k < 4; ++k) hk[k] = (u32x4){0u, 0u, 0u, 0u};
            }
        }
        SBAR();
#pragma unroll
        for (int k = 0; k < 8; ++k) {
            const int n = 2 * it + (k >> 2);
            if (n < 21) mma(n, k & 3, wf[it & 1][k]);
            if (2 * it + 2 + (k >> 2) < 21) ldk(2 * it + 2 + (k >> 2), k & 3, wf[(it + 1) & 1][k]);
            {
                constexpr int np = it == 0 ? 4 : it <= 2 ? 2 : it == 3 ? 4 : it <= 5 ? 2 : it == 6 ? 1 : 0;
                if (np == 4) { if ((k & 1) == 0 && !(it == 3)) issue_nth(it, k >> 1); if (it == 3 && k < 4) issue_nth(it, k); }
                else if (np == 2) { if ((k & 3) == 1) issue_nth(it, k >> 2); }
                else if (np == 1) { if (k == 1) issue_nth(it, 0); }
            }
            hook(it, k);
            SBAR();
        }
    };
    run_item(std::integral_constant<int, 0>{}); run_item(std::integral_constant<int, 1>{}); run_item(std::integral_constant<int, 2>{});
    run_item(std::integral_constant<int, 3>{}); run_item(std::integral_constant<int, 4>{}); run_item(std::integral_constant<int, 5>{});
    run_item(std::integral_constant<int, 6>{}); run_item(std::integral_constant<int, 7>{}); run_item(std::integral_constant<int, 8>{});
    run_item(std::integral_constant<int, 9>{}); run_item(std::integral_constant<int, 10>{});
#pragma unroll
    for (int i = 0; i < 16; ++i) accQ[i] *= SCALE_LOG2E;
    q0 = pack8(accQ, 0);
    { u32x4 e = pack8u(accQ, 1); e[2] = h ? 0u : e[2]; e[3] = h ? 0u : e[3]; q1 = __builtin_bit_cast(bf16x8, e); }

    if (hv0) WAITV(11); else WAITV(7);
    flag_store(rs, WS_FV + wg * 128 + w * 8, lane, epoch);
    LBAR();
    {
        const lptr lo = L + L_K + (j0 * 2 + hh) * 2048 + lane * 16;
        lds_w128(lo, hk[0]); lds_w128(lo + 1024, hk[1]); lds_w128(lo + 4096, hk[2]); lds_w128(lo + 4096 + 1024, hk[3]);
    }
    LBAR();
    glds16_nc(wo3 + (48 + w) * 8 * XROW, wob, lbase + L_WO0 + (48 + w) * 1024);
    const float NEG = -__builtin_inff();
    const lptr kb = L + L_K + (t * 2 + hh) * 2048 + lane * 16;
    const lptr vb = L + L_V + (t * 2 + hh) * 2048 + lane * 16;
    f32x16 o = {};
    auto attn = [&](auto EDGE_) {
        constexpr bool EDGE = decltype(EDGE_)::value;
        bf16x8 kf0[9], kf1[9];
#pragma unroll
        for (int j = 0; j < 9; ++j) { kf0[j] = lds_b128(kb + j * 4096); kf1[j] = lds_b128(kb + j * 4096 + 1024); }
        const f32x16 zero = {};
#define S_TILE(dst, j) do { dst = MFMA(kf0[j], q0, zero); dst = MFMA(kf1[j], q1, dst); } while (0)
#define MX_TILE(src, j) do { if (!(EDGE && 4 * r - 8 + t + (j) < 0)) { _Pragma("unroll") for (int i_ = 0; i_ < 16; i_ += 4) { m0 = __builtin_fmaxf(__builtin_fmaxf(m0, src[i_]), src[i_ + 1]); m1 = __builtin_fmaxf(__builtin_fmaxf(m1, src[i_ + 2]), src[i_ + 3]); } } } while (0)
        f32x16 s0, mg, y0, y1, y2;
        float m0 = NEG, m1 = NEG;
        S_TILE(s0, 0); S_TILE(mg, 8); S_TILE(y0, 1); S_TILE(y1, 2);
        SBAR();
        S_TILE(y2, 3);
        if (EDGE && 4 * r - 8 + t < 0) {
#pragma unroll
            for (int i = 0; i < 16; ++i) s0[i] = NEG;
        }
#pragma unroll
        for (int i = 0; i < 16; ++i) { const int c = (i & 3) + 8 * (i >> 2) + 4 * h; mg[i] = c > r31 ? s0[i] : mg[i]; }
#pragma unroll
        for (int i = 0; i < 16; i += 4) { m0 = __builtin_fmaxf(__builtin_fmaxf(m0, mg[i]), mg[i + 1]); m1 = __builtin_fmaxf(__builtin_fmaxf(m1, mg[i + 2]), mg[i + 3]); }
        SBAR();
        MX_TILE(y0, 1); SBAR();
        S_TILE(y0, 4); MX_TILE(y1, 2); SBAR();
        S_TILE(y1, 5); MX_TILE(y2, 3); SBAR();
        S_TILE(y2, 6); MX_TILE(y0, 4); SBAR();
        S_TILE(y0, 7); MX_TILE(y1, 5); SBAR();
        MX_TILE(y2, 6); SBAR();
        MX_TILE(y0, 7);
        float m = __builtin_fmaxf(m0, m1);
        {
            auto rr = __builtin_amdgcn_permlane32_swap(__float_as_uint(m), __float_as_uint(m), false, false);
            m = __builtin_fmaxf(__uint_as_float(rr[0]), __uint_as_float(rr[1]));
        }
        f32x16 cm;
#pragma unroll
        for (int i = 0; i < 16; ++i) cm[i] = -m;
#define SM_TILE(dst, j) do { dst = MFMA(kf0[j], q0, cm); dst = MFMA(kf1[j], q1, dst); } while (0)
        SM_TILE(y0, 7); SM_TILE(y1, 6);
        const u64 vflag = __hip_atomic_load((const GAS u64*)(ws + WS_FV + hfo), RLX_AGENT);
        bf16x8 pa[9], pb[9];
#define EX_TILE(src, j) do { if (EDGE && 4 * r - 8 + t + (j) < 0) { _Pragma("unroll") for (int i_ = 0; i_ < 16; ++i_) src[i_] = 0.f; } else { _Pragma("unroll") for (int i_ = 0; i_ < 16; ++i_) src[i_] = __builtin_amdgcn_exp2f(src[i_]); } \
        pa[j] = pack8(src, 0); pb[j] = pack8(src, 1); } while (0)
#define PV_TILE(j) do { const bf16x8 v0_ = lds_b128(vb + (j) * 4096), v1_ = lds_b128(vb + (j) * 4096 + 1024); o = MFMA(v0_, pa[j], o); o = MFMA(v1_, pb[j], o); } while (0)
        {
#pragma unroll
            for (int i = 0; i < 16; ++i) mg[i] = __builtin_amdgcn_exp2f(mg[i] - m);
#pragma unroll
            for (int i = 0; i < 16; ++i) {
                const int c = (i & 3) + 8 * (i >> 2) + 4 * h;
                s0[i] = c > r31 ? mg[i] : 0.f;
                mg[i] = c > r31 ? 0.f : mg[i];
            }
            pa[8] = pack8(mg, 0); pb[8] = pack8(mg, 1); pa[0] = pack8(s0, 0); pb[0] = pack8(s0, 1);
        }
        SBAR();
        SM_TILE(y2, 5); PV_TILE(8);
        EX_TILE(y0, 7); SBAR();
        SM_TILE(y0, 4);
        EX_TILE(y1, 6); SBAR();
        u32x4 hv[4];
        if (hv0) {
            if (!__all(lane >= 2 || vflag == epoch)) wait_flags(ws, WS_FV + hfo, lane < 2, epoch);
            asm volatile("" ::: "memory");
#pragma unroll
            for (int k = 0; k < 4; ++k) hv[k] = __builtin_amdgcn_raw_buffer_load_b128(rs, WS_V + hgo + k * 1024, 0, AUX_SC1);
        } else {
#pragma unroll
            for (int k = 0; k < 4; ++k) hv[k] = (u32x4){0u, 0u, 0u, 0u};
        }
        SM_TILE(y1, 3);
        EX_TILE(y2, 5); SBAR();
        SM_TILE(y2, 2);
        EX_TILE(y0, 4); SBAR();
        SM_TILE(y0, 1);
        EX_TILE(y1, 3);
        {
            const lptr lo = L + L_V + (j0 * 2 + hh) * 2048 + lane * 16;
            lds_w128(lo, hv[0]); lds_w128(lo + 1024, hv[1]); lds_w128(lo + 4096, hv[2]); lds_w128(lo + 4096 + 1024, hv[3]);
        }
        LBAR();
        PV_TILE(7); PV_TILE(6); PV_TILE(5);
        EX_TILE(y2, 2); SBAR();
        PV_TILE(4); PV_TILE(0); PV_TILE(3);
        EX_TILE(y0, 1); SBAR();
        PV_TILE(2); PV_TILE(1);
    };
    if (__builtin_expect(r >= 2, 1)) attn(std::false_type{}); else attn(std::true_type{});
    {
        float l = o[12];
        auto rr = __builtin_amdgcn_permlane32_swap(__float_as_uint(l), __float_as_uint(l), false, false);
        l = __uint_as_float(rr[1]);
        const float inv = 1.0f / l;
#pragma unroll
        for (int i = 0; i < 16; ++i) o[i] *= inv;
        if (h == 1) {
#pragma unroll
            for (int i = 12; i < 16; ++i) o[i] = 0.f;
        }
        const unsigned go = WS_O + (unsigned)(((b * NTILE + 4 * r + t) * NH + 2 * g + hh) * 2048) + lane * 16;
        __builtin_amdgcn_raw_buffer_store_b128(pack8u(o, 0), rs, go, 0, AUX_SC1);
        __builtin_amdgcn_raw_buffer_store_b128(pack8u(o, 1), rs, go + 1024, 0, AUX_SC1);
        if (nimg == 2) {
#pragma unroll
            for (int i = 0; i < 7; ++i) glds16_nc(wo3 + 128 + (7 * w + i) * 8 * XROW, wob, lbase + L_WO1 + (7 * w + i) * 1024);
            WAITV(7);
        } else WAITV(0);
        flag_store(rs, WS_FO + wg * 128 + w * 8, lane, epoch);
    }

    const int tt = w & 1, nt = w >> 1;
    bf16x8 wfr[32];
    const int row0 = 4 * h + q;
    const lptr pw = L + L_WO0 + (nt >> 1) * (L_WO1 - L_WO0) + row0 * 128 + ((((nt & 1) << 6) + 32 * blk + 8 * pp) ^ wsw);
    const lptr pwl = pw + (h ? (444 - 15 * DH - 24 - 4) * 128 : 0);
    auto ld_wfr = [&]() {
#pragma unroll
        for (int hd = 0; hd < 16; ++hd)
#pragma unroll
            for (int s = 0; s < 2; ++s) {
                const int ro = (hd * DH + 16 * s) * 128;
                if (hd == 15 && s == 1) wfr[2 * hd + s] = tr_frag(pw + ro, pwl + ro + 1024);
                else wfr[2 * hd + s] = tr_frag(pw + ro, pw + ro + 1024);
            }
    };
    LBAR();
    if (nt < 2) ld_wfr();
    LBAR();
    wait_flags(ws, WS_FO + (unsigned)(((b * 16 + r) * 8 + w) * 128 + ((lane & 1) * 4 + 2 * (g >> 2) + ((lane >> 1) & 1)) * 8), lane < 4, epoch);
    {
        const char* ob = ws + WS_O;
        const unsigned go = (unsigned)(((b * NTILE + 2 * rb3) * NH + 2 * w) * 2048) + lane * 16;
#pragma unroll
        for (int i = 0; i < 4; ++i) {
            glds16_sc1(go + i * 1024, ob, lbase + w * 4096 + i * 1024);
            glds16_sc1(go + NH * 2048 + i * 1024, ob, lbase + 32768 + w * 4096 + i * 1024);
        }
    }
    WAITV_BAR(8);
    if (nt >= 2) ld_wfr();
    WAITV_BAR(0);
    f32x16 fin = {};
    {
        const lptr po = L + tt * 32768 + lane * 16;
        bf16x8 ofr[8];
#pragma unroll
        for (int k = 0; k < 8; ++k) ofr[k] = lds_b128(po + k * 1024);
        SBAR();
#pragma unroll
        for (int k = 0; k < 32; ++k) {
            fin = MFMA(wfr[k], ofr[k & 7], fin);
            if (k + 8 < 32) ofr[k & 7] = lds_b128(po + (k + 8) * 1024);
            SBAR();
        }
    }
    {
        const lptr ft = L + 98304 + (32 * tt + r31) * 272 + nt * 64 + h * 8;
#pragma unroll
        for (int e = 0; e < 4; ++e) {
            typedef unsigned u32x2 __attribute__((ext_vector_type(2)));
            const u32x2 v = {pk2(fin[4 * e], fin[4 * e + 1]), pk2(fin[4 * e + 2], fin[4 * e + 3])};
            *(LAS u32x2*)(ft + e * 16) = v;
        }
    }
    LBAR();
    {
        const int row = w * 8 + (lane >> 4), ch = lane & 15;
        char* op = out + (size_t)(64 * rb3 + row) * OROW + (b * D_ + 128 * ct) * 2 + ch * 16;
        const lptr fr = L + 98304 + row * 272 + ch * 16;
        const u32x4 v0 = *(const LAS u32x4*)fr, v1 = *(const LAS u32x4*)(fr + 4 * 272);
        const __amdgpu_buffer_rsrc_t ro = __builtin_amdgcn_make_buffer_rsrc(out, 0, S_ * OROW, 0x00020000);
        const unsigned oo = ch < 8 * nimg ? (unsigned)(op - out) : 0xF0000000u;
        __builtin_amdgcn_raw_buffer_store_b128(v0, ro, oo, 0, 2);
        __builtin_amdgcn_raw_buffer_store_b128(v1, ro, oo + 4 * OROW, 0, 2);
    }
    if (wg == 0 && w == 0) {
        const u64 t0 = __builtin_amdgcn_s_memrealtime();
        for (;;) {
            bool ok = true;
#pragma unroll
            for (int k = 0; k < 4; ++k) ok &= __hip_atomic_load((const GAS u64*)(ws + WS_FO + (k * 64 + lane) * 128), RLX_AGENT) == epoch;
            if (__all(ok)) break;
            __builtin_amdgcn_s_sleep(1);
            if (__builtin_amdgcn_s_memrealtime() - t0 > 2000000ull) break;
        }
        if (lane == 0) __hip_atomic_store((GAS u64*)(ws + WS_G), epoch, RLX_AGENT);
    }
}

extern "C" void kernel_launch(void* const* d_in, const int* in_sizes, int n_in, void* d_out, int out_size, void* d_ws, size_t ws_size,
                              hipStream_t stream) {
    (void)hipFuncSetAttribute((const void*)k_fused, hipFuncAttributeMaxDynamicSharedMemorySize, LDS_BYTES);
    k_fused<<<256, 512, LDS_BYTES, stream>>>((const char*)d_in[0], (const char*)d_in[1], (const char*)d_in[2], (const char*)d_in[3],
                                             (const char*)d_in[4], (const char*)d_in[5], (char*)d_out, (char*)d_ws);
}
```

```cpp
#include <hip/hip_runtime.h>
#include <hip/hip_bf16.h>
#include <stdint.h>
#include <type_traits>

typedef __bf16 bf16x8 __attribute__((ext_vector_type(8)));
typedef __bf16 bf16x2 __attribute__((ext_vector_type(2)));
typedef float f32x16 __attribute__((ext_vector_type(16)));
typedef float f32x2 __attribute__((ext_vector_type(2)));
typedef float f32x4 __attribute__((ext_vector_type(4)));
typedef short s16x4 __attribute__((ext_vector_type(4)));
typedef short s16x8 __attribute__((ext_vector_type(8)));
typedef unsigned u32x4 __attribute__((ext_vector_type(4)));
typedef unsigned long long u64;
#define LAS __attribute__((address_space(3)))
#define GAS __attribute__((address_space(1)))
#define DI __device__ __forceinline__
#define MFMA(a, b, c) __builtin_amdgcn_mfma_f32_32x32x16_bf16((a), (b), (c), 0, 0, 0)
#define RLX_AGENT __ATOMIC_RELAXED, __HIP_MEMORY_SCOPE_AGENT

constexpr int S_ = 2048, D_ = 448, NH = 16, DH = 28;
constexpr int XROW = D_ * 2;
constexpr int WROW = 3 * D_ * 2;
constexpr int OROW = 2 * D_ * 2;
constexpr int NTILE = S_ / 32;
constexpr unsigned WS_G = 0;
constexpr unsigned WS_FK = 4096;
constexpr unsigned WS_FV = WS_FK + 256 * 128;
constexpr unsigned WS_FO = WS_FV + 256 * 128;
constexpr unsigned WS_K = 1u << 20;
constexpr unsigned KARR_BYTES = 2u * NH * NTILE * 2048;
constexpr unsigned WS_V = WS_K + KARR_BYTES;
constexpr unsigned WS_O = WS_V + KARR_BYTES;
constexpr unsigned WS_END = WS_O + KARR_BYTES;
constexpr float SCALE_LOG2E = 0.18898223650461362f * 1.4426950408889634f;
constexpr int AUX_SC1 = 16;

DI unsigned pk2(float a, float b) {
    f32x2 p = {a, b};
    return __builtin_bit_cast(unsigned, __builtin_convertvector(p, bf16x2));
}
DI u32x4 pack8u(const f32x16& x, int s) {
    u32x4 w = {pk2(x[8 * s + 0], x[8 * s + 1]), pk2(x[8 * s + 2], x[8 * s + 3]), pk2(x[8 * s + 4], x[8 * s + 5]), pk2(x[8 * s + 6], x[8 * s + 7])};
    return w;
}
DI bf16x8 pack8(const f32x16& x, int s) { return __builtin_bit_cast(bf16x8, pack8u(x, s)); }
DI void glds16(unsigned voff, const void* sbase, unsigned lds_base) {
    unsigned sv;
    asm volatile("s_mov_b32 %0, m0\n\ts_mov_b32 m0, %3\n\ts_nop 0\n\tglobal_load_lds_dwordx4 %1, %2\n\ts_mov_b32 m0, %0"
                 : "=&s"(sv) : "v"(voff), "s"(sbase), "s"(lds_base) : "memory");
}
DI void glds16_nc(unsigned voff, const void* sbase, unsigned lds_base) {
    unsigned sv;
    asm volatile("s_mov_b32 %0, m0\n\ts_mov_b32 m0, %3\n\ts_nop 0\n\tglobal_load_lds_dwordx4 %1, %2\n\ts_mov_b32 m0, %0"
                 : "=&s"(sv) : "v"(voff), "s"(sbase), "s"(lds_base));
}
DI void glds16_sc1(unsigned voff, const void* sbase, unsigned lds_base) {
    unsigned sv;
    asm volatile("s_mov_b32 %0, m0\n\ts_mov_b32 m0, %3\n\ts_nop 0\n\tglobal_load_lds_dwordx4 %1, %2 sc1\n\ts_mov_b32 m0, %0"
                 : "=&s"(sv) : "v"(voff), "s"(sbase), "s"(lds_base) : "memory");
}
typedef LAS char* lptr;
DI bf16x8 lds_b128(lptr p) { return *(const LAS bf16x8*)p; }
DI void lds_w128(lptr p, u32x4 v) { *(LAS u32x4*)p = v; }
DI s16x4 lds_tr(lptr p) { return __builtin_amdgcn_ds_read_tr16_b64_v4i16((LAS s16x4*)p); }
DI bf16x8 cat8(s16x4 lo, s16x4 hi) { s16x8 v = __builtin_shufflevector(lo, hi, 0, 1, 2, 3, 4, 5, 6, 7); return __builtin_bit_cast(bf16x8, v); }
DI bf16x8 tr_frag(lptr a0, lptr a1) { return cat8(lds_tr(a0), lds_tr(a1)); }
#define SBAR() __builtin_amdgcn_sched_barrier(0)

#define WAITV(N) asm volatile("s_waitcnt vmcnt(" #N ")" ::: "memory")
#define WAITV_BAR(N) asm volatile("s_waitcnt vmcnt(" #N ") lgkmcnt(0)\n\ts_barrier" ::: "memory")
#define LBAR() asm volatile("s_waitcnt lgkmcnt(0)\n\ts_barrier" ::: "memory")
template <int N> DI void waitv_bar() {
    if constexpr (N == 2) WAITV_BAR(2); else if constexpr (N == 3) WAITV_BAR(3); else if constexpr (N == 4) WAITV_BAR(4);
    else if constexpr (N == 6) WAITV_BAR(6); else if constexpr (N == 12) WAITV_BAR(12); else LBAR();
}

DI void flag_store(__amdgpu_buffer_rsrc_t rs, unsigned off, int lane, u64 e) {
    typedef unsigned u32x2 __attribute__((ext_vector_type(2)));
    const u32x2 v = {(unsigned)e, (unsigned)(e >> 32)};
    __builtin_amdgcn_raw_buffer_store_b64(v, rs, lane == 0 ? off : 0xF0000000u, 0, AUX_SC1);
}
DI void wait_flags(const char* ws, unsigned off, bool active, u64 e) {
    const GAS u64* p = (const GAS u64*)(ws + off);
    const u64 t0 = __builtin_amdgcn_s_memrealtime();
    for (;;) {
        u64 v = e;
        if (active) v = __hip_atomic_load(p, RLX_AGENT);
        if (__all(v == e)) break;
        __builtin_amdgcn_s_sleep(1);
        if (__builtin_amdgcn_s_memrealtime() - t0 > 2000000ull) break;
    }
    asm volatile("" ::: "memory");
}

constexpr int L_X = 0;
constexpr int L_W = 98304;
constexpr int L_V = 0;
constexpr int L_WO0 = 49152;
constexpr int L_WO1 = 106496;
constexpr int L_K = 114688;
constexpr int LDS_BYTES = 163840;
constexpr int wslot(int n) { return (n >= 12 && n < 18) ? 49152 + (n - 12) * 8192 : L_W + (n % 6) * 8192; }
constexpr int npieces(int n) { return n >= 21 ? 0 : n < 7 ? 3 : 1; }

__global__ __launch_bounds__(512, 2) void k_fused(const char* __restrict__ xA, const char* __restrict__ wA, const char* __restrict__ woA,
                                                  const char* __restrict__ xB, const char* __restrict__ wB, const char* __restrict__ woB,
                                                  char* __restrict__ out, char* __restrict__ ws) {
    extern __shared__ __attribute__((aligned(16))) char lds[];
    const u64 epoch = *(const u64*)(ws + WS_G) + 1ull;
    const int xcd = blockIdx.x & 7, idx = blockIdx.x >> 3;
    const int b = xcd >> 2;
    const int r = ((xcd >> 1) & 1) * 8 + (idx >> 2);
    const int g = (xcd & 1) * 4 + (idx & 3);
    const int wg = (b * 16 + r) * 8 + g;
    const int tid = threadIdx.x, lane = tid & 63;
    const int w = __builtin_amdgcn_readfirstlane(tid >> 6);
    const int hh = w >> 2, t = w & 3;
    const char* xb = (b ? xB : xA) + (size_t)r * 128 * XROW;
    const char* wb = (b ? wB : wA) + g * 112;
    const unsigned lbase = (unsigned)(size_t)lds;
    const lptr L = (lptr)lds;
    const __amdgpu_buffer_rsrc_t rs = __builtin_amdgcn_make_buffer_rsrc(ws, 0, WS_END, 0x00020000);

    const unsigned xo0 = (unsigned)((16 * w + (lane >> 3)) * XROW + (((lane & 7) ^ (lane >> 4)) << 4));
    const unsigned xo1 = (unsigned)((16 * w + 8 + (lane >> 3)) * XROW + (((lane & 7) ^ (4 + (lane >> 4))) << 4));
    int wc = (lane & 7) ^ (((lane >> 4) & 1) << 2); wc = wc > 6 ? 6 : wc;
    const unsigned wo0 = (unsigned)((8 * w + (lane >> 3)) * WROW + wc * 16);
    const int rot = (2 * (r & 7) + 3 * (g & 3)) % 7;

    auto issue_piece = [&](int n, int k) {
        const int pass = n / 7, tau = n % 7;
        int s = tau + rot; s = s >= 7 ? s - 7 : s;
        const unsigned poff = pass == 0 ? 896u : pass == 1 ? 1792u : 0u;
        if (k == 0) glds16(wo0 + s * 64 * WROW + poff, wb, lbase + wslot(n) + w * 1024);
        else glds16((k == 1 ? xo0 : xo1) + s * 128, xb, lbase + L_X + (n % 6) * 16384 + w * 2048 + (k - 1) * 1024);
    };
    auto issue_item = [&](int it) {
#pragma unroll
        for (int k = 0; k < npieces(2 * it); ++k) issue_piece(2 * it, k);
#pragma unroll
        for (int k = 0; k < npieces(2 * it + 1); ++k) issue_piece(2 * it + 1, k);
    };
    auto issue_nth = [&](int it, int p) {
        const int n0 = it <= 2 ? 2 * (it + 3) : it == 3 ? 12 : it <= 6 ? 2 * (it + 4) : 21;
        const int n1 = it <= 2 ? n0 + 2 : it == 3 ? 16 : it <= 6 ? n0 + 2 : 21;
        int c = 0;
#pragma unroll
        for (int n = n0; n < n1; ++n)
#pragma unroll
            for (int k = 0; k < npieces(n); ++k) { if (c == p) issue_piece(n, k); ++c; }
    };
    issue_item(0); issue_item(1); issue_item(2);

    const int r31 = lane & 31, h = lane >> 5;
    const int xrow = 32 * t + r31;
    const unsigned xa = (unsigned)(xrow * 128 + ((h ^ ((xrow >> 1) & 7)) << 4));
    lptr xp0 = L + L_X + xa, xp1 = L + L_X + (xa ^ 32), xp2 = L + L_X + (xa ^ 64), xp3 = L + L_X + (xa ^ 96);
    lptr xq0 = xp0 + 49152, xq1 = xp1 + 49152, xq2 = xp2 + 49152, xq3 = xp3 + 49152;
    asm volatile("" : "+v"(xq0), "+v"(xq1), "+v"(xq2), "+v"(xq3));
    const int blk = (lane >> 4) & 1, q = (lane & 15) >> 2, pp = lane & 3;
    const unsigned wsw = ((q >> 1) & 1) << 6;
    lptr waX = L + 49152 + (unsigned)((8 * h + q) * 128 + ((56 * hh + 32 * blk + 8 * pp) ^ wsw));
    lptr waR = waX + (L_W - 49152);
    asm volatile("" : "+v"(waX), "+v"(waR));

    bf16x8 xf[28];
    f32x16 accK, accV, accQ;
    bf16x8 q0, q1;
    const unsigned gko = (unsigned)(((b * NH + 2 * g + hh) * NTILE + 4 * r + t) * 2048) + lane * 16;
    const lptr ownK = L + L_K + ((8 + t) * 2 + hh) * 2048 + lane * 16, ownV = L + L_V + ((8 + t) * 2 + hh) * 2048 + lane * 16;
    const int j0 = (2 * w) & 7;
    const bool hv0 = r - 2 + (j0 >> 2) >= 0;
    const unsigned hgo = (unsigned)(((b * NH + 2 * g + hh) * NTILE + (hv0 ? 4 * r - 8 + j0 : 0)) * 2048) + lane * 16;
    const unsigned hfo = (unsigned)((hv0 ? wg - 8 * (2 - (j0 >> 2)) : wg) * 128 + (hh * 4 + ((j0 + (lane & 1)) & 3)) * 8);
    u32x4 hk[4];
    u64 kflag = 0;

    auto ldk = [&](int n, int ks, bf16x8& wf) {
        const int pass = n / 7, tau = n % 7;
        const lptr wbase = (wslot(n) >= L_W ? waR + (wslot(n) - L_W) : waX + (wslot(n) - 49152)) + ks * 2048;
        if (pass == 0) {
            if (n % 6 < 3) xf[4 * tau + ks] = lds_b128((ks == 0 ? xp0 : ks == 1 ? xp1 : ks == 2 ? xp2 : xp3) + (n % 6) * 16384);
            else xf[4 * tau + ks] = lds_b128((ks == 0 ? xq0 : ks == 1 ? xq1 : ks == 2 ? xq2 : xq3) + (n % 6 - 3) * 16384);
        }
        wf = tr_frag(wbase, wbase + 512);
    };
    auto mma = [&](int n, int ks, const bf16x8& wf) {
        const int pass = n / 7, tau = n % 7;
        const bool first = tau == 0 && ks == 0;
        const f32x16 zero = {};
        if (pass == 0) accK = MFMA(wf, xf[4 * tau + ks], first ? zero : accK);
        else if (pass == 1) accV = MFMA(xf[4 * tau + ks], wf, first ? zero : accV);
        else accQ = MFMA(wf, xf[4 * tau + ks], first ? zero : accQ);
    };
    u32x4 e0, e1;
    const int ct = g & 3, rb3 = 2 * r + (g >> 2);
    const int nimg = ct == 3 ? 1 : 2;
    const char* wob = (b ? woB : woA) + ct * 256;
    const unsigned wo3 = (unsigned)((lane >> 3) * XROW + (((lane & 7) ^ (((lane >> 4) & 1) << 2)) << 4));
#define WOP(i) glds16_nc(wo3 + (6 * w + (i)) * 8 * XROW, wob, lbase + L_WO0 + (6 * w + (i)) * 1024)
    auto hook = [&](int it, int k) {
        if (it == 3 && k == 5) {
            e0 = pack8u(accK, 0); e1 = pack8u(accK, 1);
            e1[2] = h ? 0u : e1[2]; e1[3] = h ? 0u : e1[3];
        }
        if (it == 3 && k == 6) { lds_w128(ownK, e0); lds_w128(ownK + 1024, e1); }
        if (it == 3 && k == 7) {
            __builtin_amdgcn_raw_buffer_store_b128(e0, rs, WS_K + gko, 0, AUX_SC1);
            __builtin_amdgcn_raw_buffer_store_b128(e1, rs, WS_K + gko + 1024, 0, AUX_SC1);
        }
        if (it == 7 && k == 0)
            flag_store(rs, WS_FK + wg * 128 + w * 8, lane, epoch);
        if (it == 7 && k == 1) {
            e0 = pack8u(accV, 0); e1 = pack8u(accV, 1);
            unsigned vv = (r31 == DH) ? 0x3F803F80u : 0u, keep = r31 < DH ? ~0u : 0u;
            asm volatile("" : "+v"(vv), "+v"(keep));
#pragma unroll
            for (int i = 0; i < 4; ++i) { e0[i] = (e0[i] & keep) | vv; e1[i] = (e1[i] & keep) | vv; }
        }
        if (it == 7 && k == 2) { lds_w128(ownV, e0); lds_w128(ownV + 1024, e1); }
        if (it == 7 && k == 3) {
            __builtin_amdgcn_raw_buffer_store_b128(e0, rs, WS_V + gko, 0, AUX_SC1);
            __builtin_amdgcn_raw_buffer_store_b128(e1, rs, WS_V + gko + 1024, 0, AUX_SC1);
        }
        if (it == 8 && (k & 1) == 1 && k >= 3) WOP((k - 3) >> 1);
        if (it == 9 && (k & 1) == 1 && k >= 3) WOP(3 + ((k - 3) >> 1));
        if (it == 9 && k == 0) kflag = __hip_atomic_load((const GAS u64*)(ws + WS_FK + hfo), RLX_AGENT);
    };

    bf16x8 wf[2][8];
    waitv_bar<12>();
#pragma unroll
    for (int k = 0; k < 8; ++k) ldk(k >> 2, k & 3, wf[0][k]);
    auto run_item = [&](auto IT) {
        constexpr int it = decltype(IT)::value;
        constexpr int cnt = it == 0 ? 6 : it == 1 ? 4 : it <= 3 ? 2 : it <= 6 ? 6 : it == 7 ? 3 : it == 8 ? 4 : it == 9 ? 6 : -1;
        waitv_bar<cnt>();
        if (it == 10) {
            if (hv0) {
                if (!__all(lane >= 2 || kflag == epoch)) wait_flags(ws, WS_FK + hfo, lane < 2, epoch);
                asm volatile("" ::: "memory");
#pragma unroll
                for (int k = 0; k < 4; ++k) hk[k] = __builtin_amdgcn_raw_buffer_load_b128(rs, WS_K + hgo + k * 1024, 0, AUX_SC1);
            } else {
#pragma unroll
                for (int k = 0; k < 4; ++k) hk[k] = (u32x4){0u, 0u, 0u, 0u};
            }
        }
        SBAR();
#pragma unroll
        for (int k = 0; k < 8; ++k) {
            const int n = 2 * it + (k >> 2);
            if (n < 21) mma(n, k & 3, wf[it & 1][k]);
            if (2 * it + 2 + (k >> 2) < 21) ldk(2 * it + 2 + (k >> 2), k & 3, wf[(it + 1) & 1][k]);
            {
                constexpr int np = it == 0 ? 4 : it <= 2 ? 2 : it == 3 ? 4 : it <= 5 ? 2 : it == 6 ? 1 : 0;
                if (np == 4) { if ((k & 1) == 0 && !(it == 3)) issue_nth(it, k >> 1); if (it == 3 && k < 4) issue_nth(it, k); }
                else if (np == 2) { if ((k & 3) == 1) issue_nth(it, k >> 2); }
                else if (np == 1) { if (k == 1) issue_nth(it, 0); }
            }
            hook(it, k);
            SBAR();
        }
    };
    run_item(std::integral_constant<int, 0>{}); run_item(std::integral_constant<int, 1>{}); run_item(std::integral_constant<int, 2>{});
    run_item(std::integral_constant<int, 3>{}); run_item(std::integral_constant<int, 4>{}); run_item(std::integral_constant<int, 5>{});
    run_item(std::integral_constant<int, 6>{}); run_item(std::integral_constant<int, 7>{}); run_item(std::integral_constant<int, 8>{});
    run_item(std::integral_constant<int, 9>{}); run_item(std::integral_constant<int, 10>{});
#pragma unroll
    for (int i = 0; i < 16; ++i) accQ[i] *= SCALE_LOG2E;
    q0 = pack8(accQ, 0);
    { u32x4 e = pack8u(accQ, 1); e[2] = h ? 0u : e[2]; e[3] = h ? 0u : e[3]; q1 = __builtin_bit_cast(bf16x8, e); }

    if (hv0) WAITV(11); else WAITV(7);
    flag_store(rs, WS_FV + wg * 128 + w * 8, lane, epoch);
    LBAR();
    {
        const lptr lo = L + L_K + (j0 * 2 + hh) * 2048 + lane * 16;
        lds_w128(lo, hk[0]); lds_w128(lo + 1024, hk[1]); lds_w128(lo + 4096, hk[2]); lds_w128(lo + 4096 + 1024, hk[3]);
    }
    LBAR();
    glds16_nc(wo3 + (48 + w) * 8 * XROW, wob, lbase + L_WO0 + (48 + w) * 1024);
    const float NEG = -__builtin_inff();
    const lptr kb = L + L_K + (t * 2 + hh) * 2048 + lane * 16;
    const lptr vb = L + L_V + (t * 2 + hh) * 2048 + lane * 16;
    f32x16 o = {};
    auto attn = [&](auto EDGE_) {
        constexpr bool EDGE = decltype(EDGE_)::value;
        bf16x8 kf0[9], kf1[9];
#pragma unroll
        for (int j = 0; j < 9; ++j) { kf0[j] = lds_b128(kb + j * 4096); kf1[j] = lds_b128(kb + j * 4096 + 1024); }
        const f32x16 zero = {};
#define S_TILE(dst, j) do { dst = MFMA(kf0[j], q0, zero); dst = MFMA(kf1[j], q1, dst); } while (0)
#define MX_TILE(src, j) do { if (!(EDGE && 4 * r - 8 + t + (j) < 0)) { _Pragma("unroll") for (int i_ = 0; i_ < 16; i_ += 4) { m0 = __builtin_fmaxf(__builtin_fmaxf(m0, src[i_]), src[i_ + 1]); m1 = __builtin_fmaxf(__builtin_fmaxf(m1, src[i_ + 2]), src[i_ + 3]); } } } while (0)
        f32x16 s0, mg, y0, y1, y2;
        float m0 = NEG, m1 = NEG;
        S_TILE(s0, 0); S_TILE(mg, 8); S_TILE(y0, 1); S_TILE(y1, 2);
        SBAR();
        S_TILE(y2, 3);
        if (EDGE && 4 * r - 8 + t < 0) {
#pragma unroll
            for (int i = 0; i < 16; ++i) s0[i] = NEG;
        }
#pragma unroll
        for (int i = 0; i < 16; ++i) { const int c = (i & 3) + 8 * (i >> 2) + 4 * h; mg[i] = c > r31 ? s0[i] : mg[i]; }
#pragma unroll
        for (int i = 0; i < 16; i += 4) { m0 = __builtin_fmaxf(__builtin_fmaxf(m0, mg[i]), mg[i + 1]); m1 = __builtin_fmaxf(__builtin_fmaxf(m1, mg[i + 2]), mg[i + 3]); }
        SBAR();
        MX_TILE(y0, 1); SBAR();
        S_TILE(y0, 4); MX_TILE(y1, 2); SBAR();
        S_TILE(y1, 5); MX_TILE(y2, 3); SBAR();
        S_TILE(y2, 6); MX_TILE(y0, 4); SBAR();
        S_TILE(y0, 7); MX_TILE(y1, 5); SBAR();
        MX_TILE(y2, 6); SBAR();
        MX_TILE(y0, 7);
        float m = __builtin_fmaxf(m0, m1);
        {
            auto rr = __builtin_amdgcn_permlane32_swap(__float_as_uint(m), __float_as_uint(m), false, false);
            m = __builtin_fmaxf(__uint_as_float(rr[0]), __uint_as_float(rr[1]));
        }
        f32x16 cm;
#pragma unroll
        for (int i = 0; i < 16; ++i) cm[i] = -m;
#define SM_TILE(dst, j) do { dst = MFMA(kf0[j], q0, cm); dst = MFMA(kf1[j], q1, dst); } while (0)
        SM_TILE(y0, 7); SM_TILE(y1, 6);
        const u64 vflag = __hip_atomic_load((const GAS u64*)(ws + WS_FV + hfo), RLX_AGENT);
        bf16x8 pa[9], pb[9];
#define EX_TILE(src, j) do { if (EDGE && 4 * r - 8 + t + (j) < 0) { _Pragma("unroll") for (int i_ = 0; i_ < 16; ++i_) src[i_] = 0.f; } else { _Pragma("unroll") for (int i_ = 0; i_ < 16; ++i_) src[i_] = __builtin_amdgcn_exp2f(src[i_]); } \
        pa[j] = pack8(src, 0); pb[j] = pack8(src, 1); } while (0)
#define PV_TILE(j) do { const bf16x8 v0_ = lds_b128(vb + (j) * 4096), v1_ = lds_b128(vb + (j) * 4096 + 1024); o = MFMA(v0_, pa[j], o); o = MFMA(v1_, pb[j], o); } while (0)
        {
#pragma unroll
            for (int i = 0; i < 16; ++i) mg[i] = __builtin_amdgcn_exp2f(mg[i] - m);
#pragma unroll
            for (int i = 0; i < 16; ++i) {
                const int c = (i & 3) + 8 * (i >> 2) + 4 * h;
                s0[i] = c > r31 ? mg[i] : 0.f;
                mg[i] = c > r31 ? 0.f : mg[i];
            }
            pa[8] = pack8(mg, 0); pb[8] = pack8(mg, 1); pa[0] = pack8(s0, 0); pb[0] = pack8(s0, 1);
        }
        SBAR();
        SM_TILE(y2, 5); PV_TILE(8);
        EX_TILE(y0, 7); SBAR();
        SM_TILE(y0, 4);
        EX_TILE(y1, 6); SBAR();
        u32x4 hv[4];
        if (hv0) {
            if (!__all(lane >= 2 || vflag == epoch)) wait_flags(ws, WS_FV + hfo, lane < 2, epoch);
            asm volatile("" ::: "memory");
#pragma unroll
            for (int k = 0; k < 4; ++k) hv[k] = __builtin_amdgcn_raw_buffer_load_b128(rs, WS_V + hgo + k * 1024, 0, AUX_SC1);
        } else {
#pragma unroll
            for (int k = 0; k < 4; ++k) hv[k] = (u32x4){0u, 0u, 0u, 0u};
        }
        SM_TILE(y1, 3);
        EX_TILE(y2, 5); SBAR();
        SM_TILE(y2, 2);
        EX_TILE(y0, 4); SBAR();
        SM_TILE(y0, 1);
        EX_TILE(y1, 3);
        {
            const lptr lo = L + L_V + (j0 * 2 + hh) * 2048 + lane * 16;
            lds_w128(lo, hv[0]); lds_w128(lo + 1024, hv[1]); lds_w128(lo + 4096, hv[2]); lds_w128(lo + 4096 + 1024, hv[3]);
        }
        LBAR();
        PV_TILE(7); PV_TILE(6); PV_TILE(5);
        EX_TILE(y2, 2); SBAR();
        PV_TILE(4); PV_TILE(0); PV_TILE(3);
        EX_TILE(y0, 1); SBAR();
        PV_TILE(2); PV_TILE(1);
    };
    if (__builtin_expect(r >= 2, 1)) attn(std::false_type{}); else attn(std::true_type{});
    {
        float l = o[12];
        auto rr = __builtin_amdgcn_permlane32_swap(__float_as_uint(l), __float_as_uint(l), false, false);
        l = __uint_as_float(rr[1]);
        const float inv = 1.0f / l;
#pragma unroll
        for (int i = 0; i < 16; ++i) o[i] *= inv;
        if (h == 1) {
#pragma unroll
            for (int i = 12; i < 16; ++i) o[i] = 0.f;
        }
        const unsigned go = WS_O + (unsigned)(((b * NTILE + 4 * r + t) * NH + 2 * g + hh) * 2048) + lane * 16;
        __builtin_amdgcn_raw_buffer_store_b128(pack8u(o, 0), rs, go, 0, AUX_SC1);
        __builtin_amdgcn_raw_buffer_store_b128(pack8u(o, 1), rs, go + 1024, 0, AUX_SC1);
        if (nimg == 2) {
#pragma unroll
            for (int i = 0; i < 7; ++i) glds16_nc(wo3 + 128 + (7 * w + i) * 8 * XROW, wob, lbase + L_WO1 + (7 * w + i) * 1024);
            WAITV(7);
        } else WAITV(0);
        flag_store(rs, WS_FO + wg * 128 + w * 8, lane, epoch);
    }

    const int tt = w & 1, nt = w >> 1;
    bf16x8 wfr[32];
    const int row0 = 4 * h + q;
    const lptr pw = L + L_WO0 + (nt >> 1) * (L_WO1 - L_WO0) + row0 * 128 + ((((nt & 1) << 6) + 32 * blk + 8 * pp) ^ wsw);
    const lptr pwl = pw + (h ? (444 - 15 * DH - 24 - 4) * 128 : 0);
    auto ld_wfr = [&]() {
#pragma unroll
        for (int hd = 0; hd < 16; ++hd)
#pragma unroll
            for (int s = 0; s < 2; ++s) {
                const int ro = (hd * DH + 16 * s) * 128;
                if (hd == 15 && s == 1) wfr[2 * hd + s] = tr_frag(pw + ro, pwl + ro + 1024);
                else wfr[2 * hd + s] = tr_frag(pw + ro, pw + ro + 1024);
            }
    };
    LBAR();
    if (nt < 2) ld_wfr();
    LBAR();
    wait_flags(ws, WS_FO + (unsigned)(((b * 16 + r) * 8 + w) * 128 + ((lane & 1) * 4 + 2 * (g >> 2) + ((lane >> 1) & 1)) * 8), lane < 4, epoch);
    {
        const char* ob = ws + WS_O;
        const unsigned go = (unsigned)(((b * NTILE + 2 * rb3) * NH + 2 * w) * 2048) + lane * 16;
#pragma unroll
        for (int i = 0; i < 4; ++i) {
            glds16_sc1(go + i * 1024, ob, lbase + w * 4096 + i * 1024);
            glds16_sc1(go + NH * 2048 + i * 1024, ob, lbase + 32768 + w * 4096 + i * 1024);
        }
    }
    WAITV_BAR(8);
    if (nt >= 2) ld_wfr();
    WAITV_BAR(0);
    f32x16 fin = {};
    {
        const lptr po = L + tt * 32768 + lane * 16;
        bf16x8 ofr[8];
#pragma unroll
        for (int k = 0; k < 8; ++k) ofr[k] = lds_b128(po + k * 1024);
        SBAR();
#pragma unroll
        for (int k = 0; k < 32; ++k) {
            fin = MFMA(wfr[k], ofr[k & 7], fin);
            if (k + 8 < 32) ofr[k & 7] = lds_b128(po + (k + 8) * 1024);
            SBAR();
        }
    }
    {
        const lptr ft = L + 98304 + (32 * tt + r31) * 272 + nt * 64 + h * 8;
#pragma unroll
        for (int e = 0; e < 4; ++e) {
            typedef unsigned u32x2 __attribute__((ext_vector_type(2)));
            const u32x2 v = {pk2(fin[4 * e], fin[4 * e + 1]), pk2(fin[4 * e + 2], fin[4 * e + 3])};
            *(LAS u32x2*)(ft + e * 16) = v;
        }
    }
    LBAR();
    {
        const int row = w * 8 + (lane >> 4), ch = lane & 15;
        char* op = out + (size_t)(64 * rb3 + row) * OROW + (b * D_ + 128 * ct) * 2 + ch * 16;
        const lptr fr = L + 98304 + row * 272 + ch * 16;
        const u32x4 v0 = *(const LAS u32x4*)fr, v1 = *(const LAS u32x4*)(fr + 4 * 272);
        const __amdgpu_buffer_rsrc_t ro = __builtin_amdgcn_make_buffer_rsrc(out, 0, S_ * OROW, 0x00020000);
        const unsigned oo = ch < 8 * nimg ? (unsigned)(op - out) : 0xF0000000u;
        __builtin_amdgcn_raw_buffer_store_b128(v0, ro, oo, 0, AUX_SC1);
        __builtin_amdgcn_raw_buffer_store_b128(v1, ro, oo + 4 * OROW, 0, AUX_SC1);
    }
    if (wg == 0 && w == 0) {
        const u64 t0 = __builtin_amdgcn_s_memrealtime();
        for (;;) {
            bool ok = true;
#pragma unroll
            for (int k = 0; k < 4; ++k) ok &= __hip_atomic_load((const GAS u64*)(ws + WS_FO + (k * 64 + lane) * 128), RLX_AGENT) == epoch;
            if (__all(ok)) break;
            __builtin_amdgcn_s_sleep(1);
            if (__builtin_amdgcn_s_memrealtime() - t0 > 2000000ull) break;
        }
        if (lane == 0) __hip_atomic_store((GAS u64*)(ws + WS_G), epoch, RLX_AGENT);
    }
}

extern "C" void kernel_launch(void* const* d_in, const int* in_sizes, int n_in, void* d_out, int out_size, void* d_ws, size_t ws_size,
                              hipStream_t stream) {
    (void)hipFuncSetAttribute((const void*)k_fused, hipFuncAttributeMaxDynamicSharedMemorySize, LDS_BYTES);
    k_fused<<<256, 512, LDS_BYTES, stream>>>((const char*)d_in[0], (const char*)d_in[1], (const char*)d_in[2], (const char*)d_in[3],
                                             (const char*)d_in[4], (const char*)d_in[5], (char*)d_out, (char*)d_ws);
}
```

```cpp
#include <hip/hip_runtime.h>
#include <hip/hip_bf16.h>
#include <stdint.h>
#include <type_traits>

typedef __bf16 bf16x8 __attribute__((ext_vector_type(8)));
typedef __bf16 bf16x2 __attribute__((ext_vector_type(2)));
typedef float f32x16 __attribute__((ext_vector_type(16)));
typedef float f32x2 __attribute__((ext_vector_type(2)));
typedef float f32x4 __attribute__((ext_vector_type(4)));
typedef short s16x4 __attribute__((ext_vector_type(4)));
typedef short s16x8 __attribute__((ext_vector_type(8)));
typedef unsigned u32x4 __attribute__((ext_vector_type(4)));
typedef unsigned long long u64;
#define LAS __attribute__((address_space(3)))
#define GAS __attribute__((address_space(1)))
#define DI __device__ __forceinline__
#define MFMA(a, b, c) __builtin_amdgcn_mfma_f32_32x32x16_bf16((a), (b), (c), 0, 0, 0)
#define RLX_AGENT __ATOMIC_RELAXED, __HIP_MEMORY_SCOPE_AGENT

constexpr int S_ = 2048, D_ = 448, NH = 16, DH = 28;
constexpr int XROW = D_ * 2;
constexpr int WROW = 3 * D_ * 2;
constexpr int OROW = 2 * D_ * 2;
constexpr int NTILE = S_ / 32;
constexpr unsigned WS_G = 0;
constexpr unsigned WS_FK = 4096;
constexpr unsigned WS_FV = WS_FK + 256 * 128;
constexpr unsigned WS_FO = WS_FV + 256 * 128;
constexpr unsigned WS_K = 1u << 20;
constexpr unsigned KARR_BYTES = 2u * NH * NTILE * 2048;
constexpr unsigned WS_V = WS_K + KARR_BYTES;
constexpr unsigned WS_O = WS_V + KARR_BYTES;
constexpr unsigned WS_END = WS_O + KARR_BYTES;
constexpr float SCALE_LOG2E = 0.18898223650461362f * 1.4426950408889634f;
constexpr int AUX_SC1 = 16;

DI unsigned pk2(float a, float b) {
    f32x2 p = {a, b};
    return __builtin_bit_cast(unsigned, __builtin_convertvector(p, bf16x2));
}
DI u32x4 pack8u(const f32x16& x, int s) {
    u32x4 w = {pk2(x[8 * s + 0], x[8 * s + 1]), pk2(x[8 * s + 2], x[8 * s + 3]), pk2(x[8 * s + 4], x[8 * s + 5]), pk2(x[8 * s + 6], x[8 * s + 7])};
    return w;
}
DI bf16x8 pack8(const f32x16& x, int s) { return __builtin_bit_cast(bf16x8, pack8u(x, s)); }
DI void glds16(unsigned voff, const void* sbase, unsigned lds_base) {
    unsigned sv;
    asm volatile("s_mov_b32 %0, m0\n\ts_mov_b32 m0, %3\n\ts_nop 0\n\tglobal_load_lds_dwordx4 %1, %2\n\ts_mov_b32 m0, %0"
                 : "=&s"(sv) : "v"(voff), "s"(sbase), "s"(lds_base) : "memory");
}
DI void glds16_nc(unsigned voff, const void* sbase, unsigned lds_base) {
    unsigned sv;
    asm volatile("s_mov_b32 %0, m0\n\ts_mov_b32 m0, %3\n\ts_nop 0\n\tglobal_load_lds_dwordx4 %1, %2\n\ts_mov_b32 m0, %0"
                 : "=&s"(sv) : "v"(voff), "s"(sbase), "s"(lds_base));
}
DI void glds16_sc1(unsigned voff, const void* sbase, unsigned lds_base) {
    unsigned sv;
    asm volatile("s_mov_b32 %0, m0\n\ts_mov_b32 m0, %3\n\ts_nop 0\n\tglobal_load_lds_dwordx4 %1, %2 sc1\n\ts_mov_b32 m0, %0"
                 : "=&s"(sv) : "v"(voff), "s"(sbase), "s"(lds_base) : "memory");
}
typedef LAS char* lptr;
DI bf16x8 lds_b128(lptr p) { return *(const LAS bf16x8*)p; }
DI void lds_w128(lptr p, u32x4 v) { *(LAS u32x4*)p = v; }
DI s16x4 lds_tr(lptr p) { return __builtin_amdgcn_ds_read_tr16_b64_v4i16((LAS s16x4*)p); }
DI bf16x8 cat8(s16x4 lo, s16x4 hi) { s16x8 v = __builtin_shufflevector(lo, hi, 0, 1, 2, 3, 4, 5, 6, 7); return __builtin_bit_cast(bf16x8, v); }
DI bf16x8 tr_frag(lptr a0, lptr a1) { return cat8(lds_tr(a0), lds_tr(a1)); }
#define SBAR() __builtin_amdgcn_sched_barrier(0)

#define WAITV(N) asm volatile("s_waitcnt vmcnt(" #N ")" ::: "memory")
#define WAITV_BAR(N) asm volatile("s_waitcnt vmcnt(" #N ") lgkmcnt(0)\n\ts_barrier" ::: "memory")
#define LBAR() asm volatile("s_waitcnt lgkmcnt(0)\n\ts_barrier" ::: "memory")
template <int N> DI void waitv_bar() {
    if constexpr (N == 2) WAITV_BAR(2); else if constexpr (N == 3) WAITV_BAR(3); else if constexpr (N == 4) WAITV_BAR(4);
    else if constexpr (N == 6) WAITV_BAR(6); else if constexpr (N == 12) WAITV_BAR(12); else LBAR();
}

DI void flag_store(__amdgpu_buffer_rsrc_t rs, unsigned off, int lane, u64 e) {
    typedef unsigned u32x2 __attribute__((ext_vector_type(2)));
    const u32x2 v = {(unsigned)e, (unsigned)(e >> 32)};
    __builtin_amdgcn_raw_buffer_store_b64(v, rs, lane == 0 ? off : 0xF0000000u, 0, AUX_SC1);
}
DI void wait_flags(const char* ws, unsigned off, bool active, u64 e) {
    const GAS u64* p = (const GAS u64*)(ws + off);
    const u64 t0 = __builtin_amdgcn_s_memrealtime();
    for (;;) {
        u64 v = e;
        if (active) v = __hip_atomic_load(p, RLX_AGENT);
        if (__all(v == e)) break;
        __builtin_amdgcn_s_sleep(1);
        if (__builtin_amdgcn_s_memrealtime() - t0 > 2000000ull) break;
    }
    asm volatile("" ::: "memory");
}

constexpr int L_X = 0;
constexpr int L_W = 98304;
constexpr int L_V = 0;
constexpr int L_WO0 = 49152;
constexpr int L_WO1 = 106496;
constexpr int L_K = 114688;
constexpr int LDS_BYTES = 163840;
constexpr int wslot(int n) { return (n >= 12 && n < 18) ? 49152 + (n - 12) * 8192 : L_W + (n % 6) * 8192; }
constexpr int npieces(int n) { return n >= 21 ? 0 : n < 7 ? 3 : 1; }

__global__ __launch_bounds__(512, 2) void k_fused(const char* __restrict__ xA, const char* __restrict__ wA, const char* __restrict__ woA,
                                                  const char* __restrict__ xB, const char* __restrict__ wB, const char* __restrict__ woB,
                                                  char* __restrict__ out, char* __restrict__ ws) {
    extern __shared__ __attribute__((aligned(16))) char lds[];
    const u64 epoch = *(const u64*)(ws + WS_G) + 1ull;
    const int xcd = blockIdx.x & 7, idx = blockIdx.x >> 3;
    const int b = xcd >> 2;
    const int r = ((xcd >> 1) & 1) * 8 + (idx >> 2);
    const int g = (xcd & 1) * 4 + (idx & 3);
    const int wg = (b * 16 + r) * 8 + g;
    const int tid = threadIdx.x, lane = tid & 63;
    const int w = __builtin_amdgcn_readfirstlane(tid >> 6);
    const int hh = w >> 2, t = w & 3;
    const char* xb = (b ? xB : xA) + (size_t)r * 128 * XROW;
    const char* wb = (b ? wB : wA) + g * 112;
    const unsigned lbase = (unsigned)(size_t)lds;
    const lptr L = (lptr)lds;
    const __amdgpu_buffer_rsrc_t rs = __builtin_amdgcn_make_buffer_rsrc(ws, 0, WS_END, 0x00020000);

    const unsigned xo0 = (unsigned)((16 * w + (lane >> 3)) * XROW + (((lane & 7) ^ (lane >> 4)) << 4));
    const unsigned xo1 = (unsigned)((16 * w + 8 + (lane >> 3)) * XROW + (((lane & 7) ^ (4 + (lane >> 4))) << 4));
    int wc = (lane & 7) ^ (((lane >> 4) & 1) << 2); wc = wc > 6 ? 6 : wc;
    const unsigned wo0 = (unsigned)((8 * w + (lane >> 3)) * WROW + wc * 16);
    const int rot = (2 * (r & 7) + 3 * (g & 3)) % 7;

    auto issue_piece = [&](int n, int k) {
        const int pass = n / 7, tau = n % 7;
        int s = tau + rot; s = s >= 7 ? s - 7 : s;
        const unsigned poff = pass == 0 ? 896u : pass == 1 ? 1792u : 0u;
        if (k == 0) glds16(wo0 + s * 64 * WROW + poff, wb, lbase + wslot(n) + w * 1024);
        else glds16((k == 1 ? xo0 : xo1) + s * 128, xb, lbase + L_X + (n % 6) * 16384 + w * 2048 + (k - 1) * 1024);
    };
    auto issue_item = [&](int it) {
#pragma unroll
        for (int k = 0; k < npieces(2 * it); ++k) issue_piece(2 * it, k);
#pragma unroll
        for (int k = 0; k < npieces(2 * it + 1); ++k) issue_piece(2 * it + 1, k);
    };
    auto issue_nth = [&](int it, int p) {
        const int n0 = it <= 2 ? 2 * (it + 3) : it == 3 ? 12 : it <= 6 ? 2 * (it + 4) : 21;
        const int n1 = it <= 2 ? n0 + 2 : it == 3 ? 16 : it <= 6 ? n0 + 2 : 21;
        int c = 0;
#pragma unroll
        for (int n = n0; n < n1; ++n)
#pragma unroll
            for (int k = 0; k < npieces(n); ++k) { if (c == p) issue_piece(n, k); ++c; }
    };
    issue_item(0); issue_item(1); issue_item(2);

    const int r31 = lane & 31, h = lane >> 5;
    const int xrow = 32 * t + r31;
    const unsigned xa = (unsigned)(xrow * 128 + ((h ^ ((xrow >> 1) & 7)) << 4));
    lptr xp0 = L + L_X + xa, xp1 = L + L_X + (xa ^ 32), xp2 = L + L_X + (xa ^ 64), xp3 = L + L_X + (xa ^ 96);
    lptr xq0 = xp0 + 49152, xq1 = xp1 + 49152, xq2 = xp2 + 49152, xq3 = xp3 + 49152;
    asm volatile("" : "+v"(xq0), "+v"(xq1), "+v"(xq2), "+v"(xq3));
    const int blk = (lane >> 4) & 1, q = (lane & 15) >> 2, pp = lane & 3;
    const unsigned wsw = ((q >> 1) & 1) << 6;
    lptr waX = L + 49152 + (unsigned)((8 * h + q) * 128 + ((56 * hh + 32 * blk + 8 * pp) ^ wsw));
    lptr waR = waX + (L_W - 49152);
    asm volatile("" : "+v"(waX), "+v"(waR));

    bf16x8 xf[28];
    f32x16 accK, accV, accQ;
    bf16x8 q0, q1;
    const unsigned gko = (unsigned)(((b * NH + 2 * g + hh) * NTILE + 4 * r + t) * 2048) + lane * 16;
    const lptr ownK = L + L_K + ((8 + t) * 2 + hh) * 2048 + lane * 16, ownV = L + L_V + ((8 + t) * 2 + hh) * 2048 + lane * 16;
    const int j0 = (2 * w) & 7;
    const bool hv0 = r - 2 + (j0 >> 2) >= 0;
    const unsigned hgo = (unsigned)(((b * NH + 2 * g + hh) * NTILE + (hv0 ? 4 * r - 8 + j0 : 0)) * 2048) + lane * 16;
    const unsigned hfo = (unsigned)((hv0 ? wg - 8 * (2 - (j0 >> 2)) : wg) * 128 + (hh * 4 + ((j0 + (lane & 1)) & 3)) * 8);
    u32x4 hk[4];
    u64 kflag = 0;

    auto ldk = [&](int n, int ks, bf16x8& wf) {
        const int pass = n / 7, tau = n % 7;
        const lptr wbase = (wslot(n) >= L_W ? waR + (wslot(n) - L_W) : waX + (wslot(n) - 49152)) + ks * 2048;
        if (pass == 0) {
            if (n % 6 < 3) xf[4 * tau + ks] = lds_b128((ks == 0 ? xp0 : ks == 1 ? xp1 : ks == 2 ? xp2 : xp3) + (n % 6) * 16384);
            else xf[4 * tau + ks] = lds_b128((ks == 0 ? xq0 : ks == 1 ? xq1 : ks == 2 ? xq2 : xq3) + (n % 6 - 3) * 16384);
        }
        wf = tr_frag(wbase, wbase + 512);
    };
    auto mma = [&](int n, int ks, const bf16x8& wf) {
        const int pass = n / 7, tau = n % 7;
        const bool first = tau == 0 && ks == 0;
        const f32x16 zero = {};
        if (pass == 0) accK = MFMA(wf, xf[4 * tau + ks], first ? zero : accK);
        else if (pass == 1) accV = MFMA(xf[4 * tau + ks], wf, first ? zero : accV);
        else accQ = MFMA(wf, xf[4 * tau + ks], first ? zero : accQ);
    };
    u32x4 e0, e1;
    const int ct = g & 3, rb3 = 2 * r + (g >> 2);
    const int nimg = ct == 3 ? 1 : 2;
    const char* wob = (b ? woB : woA) + ct * 256;
    const unsigned wo3 = (unsigned)((lane >> 3) * XROW + (((lane & 7) ^ (((lane >> 4) & 1) << 2)) << 4));
#define WOP(i) glds16_nc(wo3 + (6 * w + (i)) * 8 * XROW, wob, lbase + L_WO0 + (6 * w + (i)) * 1024)
    auto hook = [&](int it, int k) {
        if (it == 3 && k == 5) {
            e0 = pack8u(accK, 0); e1 = pack8u(accK, 1);
            e1[2] = h ? 0u : e1[2]; e1[3] = h ? 0u : e1[3];
        }
        if (it == 3 && k == 6) { lds_w128(ownK, e0); lds_w128(ownK + 1024, e1); }
        if (it == 3 && k == 7) {
            __builtin_amdgcn_raw_buffer_store_b128(e0, rs, WS_K + gko, 0, AUX_SC1);
            __builtin_amdgcn_raw_buffer_store_b128(e1, rs, WS_K + gko + 1024, 0, AUX_SC1);
        }
        if (it == 7 && k == 0)
            flag_store(rs, WS_FK + wg * 128 + w * 8, lane, epoch);
        if (it == 7 && k == 1) {
            e0 = pack8u(accV, 0); e1 = pack8u(accV, 1);
            unsigned vv = (r31 == DH) ? 0x3F803F80u : 0u, keep = r31 < DH ? ~0u : 0u;
            asm volatile("" : "+v"(vv), "+v"(keep));
#pragma unroll
            for (int i = 0; i < 4; ++i) { e0[i] = (e0[i] & keep) | vv; e1[i] = (e1[i] & keep) | vv; }
        }
        if (it == 7 && k == 2) { lds_w128(ownV, e0); lds_w128(ownV + 1024, e1); }
        if (it == 7 && k == 3) {
            __builtin_amdgcn_raw_buffer_store_b128(e0, rs, WS_V + gko, 0, AUX_SC1);
            __builtin_amdgcn_raw_buffer_store_b128(e1, rs, WS_V + gko + 1024, 0, AUX_SC1);
        }
        if (it == 8 && (k & 1) == 1 && k >= 3) WOP((k - 3) >> 1);
        if (it == 9 && (k & 1) == 1 && k >= 3) WOP(3 + ((k - 3) >> 1));
        if (it == 9 && k == 0) kflag = __hip_atomic_load((const GAS u64*)(ws + WS_FK + hfo), RLX_AGENT);
    };

    bf16x8 wf[2][8];
    waitv_bar<12>();
#pragma unroll
    for (int k = 0; k < 8; ++k) ldk(k >> 2, k & 3, wf[0][k]);
    auto run_item = [&](auto IT) {
        constexpr int it = decltype(IT)::value;
        constexpr int cnt = it == 0 ? 6 : it == 1 ? 4 : it <= 3 ? 2 : it <= 6 ? 6 : it == 7 ? 3 : it == 8 ? 4 : it == 9 ? 6 : -1;
        waitv_bar<cnt>();
        if (it == 10) {
            if (hv0) {
                if (!__all(lane >= 2 || kflag == epoch)) wait_flags(ws, WS_FK + hfo, lane < 2, epoch);
                asm volatile("" ::: "memory");
#pragma unroll
                for (int k = 0; k < 4; ++k) hk[k] = __builtin_amdgcn_raw_buffer_load_b128(rs, WS_K + hgo + k * 1024, 0, AUX_SC1);
            } else {
#pragma unroll
                for (int k = 0; k < 4; ++k) hk[k] = (u32x4){0u, 0u, 0u, 0u};
            }
        }
        SBAR();
#pragma unroll
        for (int k = 0; k < 8; ++k) {
            const int n = 2 * it + (k >> 2);
            if (n < 21) mma(n, k & 3, wf[it & 1][k]);
            if (2 * it + 2 + (k >> 2) < 21) ldk(2 * it + 2 + (k >> 2), k & 3, wf[(it + 1) & 1][k]);
            {
                constexpr int np = it == 0 ? 4 : it <= 2 ? 2 : it == 3 ? 4 : it <= 5 ? 2 : it == 6 ? 1 : 0;
                if (np == 4) { if ((k & 1) == 0 && !(it == 3)) issue_nth(it, k >> 1); if (it == 3 && k < 4) issue_nth(it, k); }
                else if (np == 2) { if ((k & 3) == 1) issue_nth(it, k >> 2); }
                else if (np == 1) { if (k == 1) issue_nth(it, 0); }
            }
            hook(it, k);
            SBAR();
        }
    };
    run_item(std::integral_constant<int, 0>{}); run_item(std::integral_constant<int, 1>{}); run_item(std::integral_constant<int, 2>{});
    run_item(std::integral_constant<int, 3>{}); run_item(std::integral_constant<int, 4>{}); run_item(std::integral_constant<int, 5>{});
    run_item(std::integral_constant<int, 6>{}); run_item(std::integral_constant<int, 7>{}); run_item(std::integral_constant<int, 8>{});
    run_item(std::integral_constant<int, 9>{}); run_item(std::integral_constant<int, 10>{});
#pragma unroll
    for (int i = 0; i < 16; ++i) accQ[i] *= SCALE_LOG2E;
    q0 = pack8(accQ, 0);
    { u32x4 e = pack8u(accQ, 1); e[2] = h ? 0u : e[2]; e[3] = h ? 0u : e[3]; q1 = __builtin_bit_cast(bf16x8, e); }

    if (hv0) WAITV(11); else WAITV(7);
    flag_store(rs, WS_FV + wg * 128 + w * 8, lane, epoch);
    LBAR();
    {
        const lptr lo = L + L_K + (j0 * 2 + hh) * 2048 + lane * 16;
        lds_w128(lo, hk[0]); lds_w128(lo + 1024, hk[1]); lds_w128(lo + 4096, hk[2]); lds_w128(lo + 4096 + 1024, hk[3]);
    }
    LBAR();
    glds16_nc(wo3 + (48 + w) * 8 * XROW, wob, lbase + L_WO0 + (48 + w) * 1024);
    const float NEG = -__builtin_inff();
    const lptr kb = L + L_K + (t * 2 + hh) * 2048 + lane * 16;
    const lptr vb = L + L_V + (t * 2 + hh) * 2048 + lane * 16;
    f32x16 o = {};
    auto attn = [&](auto EDGE_) {
        constexpr bool EDGE = decltype(EDGE_)::value;
        const u64 vflag = __hip_atomic_load((const GAS u64*)(ws + WS_FV + hfo), RLX_AGENT);
        bf16x8 kf0[9], kf1[9];
#pragma unroll
        for (int j = 0; j < 9; ++j) { kf0[j] = lds_b128(kb + j * 4096); kf1[j] = lds_b128(kb + j * 4096 + 1024); }
        const f32x16 zero = {};
#define S_TILE(dst, j) do { dst = MFMA(kf0[j], q0, zero); dst = MFMA(kf1[j], q1, dst); } while (0)
#define MX_TILE(src, j) do { if (!(EDGE && 4 * r - 8 + t + (j) < 0)) { _Pragma("unroll") for (int i_ = 0; i_ < 16; i_ += 4) { m0 = __builtin_fmaxf(__builtin_fmaxf(m0, src[i_]), src[i_ + 1]); m1 = __builtin_fmaxf(__builtin_fmaxf(m1, src[i_ + 2]), src[i_ + 3]); } } } while (0)
        f32x16 s0, mg, y0, y1, y2;
        float m0 = NEG, m1 = NEG;
        S_TILE(s0, 0); S_TILE(mg, 8); S_TILE(y0, 1); S_TILE(y1, 2);
        SBAR();
        S_TILE(y2, 3);
        if (EDGE && 4 * r - 8 + t < 0) {
#pragma unroll
            for (int i = 0; i < 16; ++i) s0[i] = NEG;
        }
#pragma unroll
        for (int i = 0; i < 16; ++i) { const int c = (i & 3) + 8 * (i >> 2) + 4 * h; mg[i] = c > r31 ? s0[i] : mg[i]; }
#pragma unroll
        for (int i = 0; i < 16; i += 4) { m0 = __builtin_fmaxf(__builtin_fmaxf(m0, mg[i]), mg[i + 1]); m1 = __builtin_fmaxf(__builtin_fmaxf(m1, mg[i + 2]), mg[i + 3]); }
        SBAR();
        MX_TILE(y0, 1); SBAR();
        S_TILE(y0, 4); MX_TILE(y1, 2); SBAR();
        S_TILE(y1, 5); MX_TILE(y2, 3); SBAR();
        S_TILE(y2, 6); MX_TILE(y0, 4); SBAR();
        S_TILE(y0, 7); MX_TILE(y1, 5); SBAR();
        MX_TILE(y2, 6); SBAR();
        MX_TILE(y0, 7);
        float m = __builtin_fmaxf(m0, m1);
        {
            auto rr = __builtin_amdgcn_permlane32_swap(__float_as_uint(m), __float_as_uint(m), false, false);
            m = __builtin_fmaxf(__uint_as_float(rr[0]), __uint_as_float(rr[1]));
        }
        f32x16 cm;
#pragma unroll
        for (int i = 0; i < 16; ++i) cm[i] = -m;
#define SM_TILE(dst, j) do { dst = MFMA(kf0[j], q0, cm); dst = MFMA(kf1[j], q1, dst); } while (0)
        SM_TILE(y0, 7); SM_TILE(y1, 6);
        u32x4 hv[4];
        if (hv0) {
            if (!__all(lane >= 2 || vflag == epoch)) wait_flags(ws, WS_FV + hfo, lane < 2, epoch);
            asm volatile("" ::: "memory");
#pragma unroll
            for (int k = 0; k < 4; ++k) hv[k] = __builtin_amdgcn_raw_buffer_load_b128(rs, WS_V + hgo + k * 1024, 0, AUX_SC1);
        } else {
#pragma unroll
            for (int k = 0; k < 4; ++k) hv[k] = (u32x4){0u, 0u, 0u, 0u};
        }
        bf16x8 pa[9], pb[9];
#define EX_TILE(src, j) do { if (EDGE && 4 * r - 8 + t + (j) < 0) { _Pragma("unroll") for (int i_ = 0; i_ < 16; ++i_) src[i_] = 0.f; } else { _Pragma("unroll") for (int i_ = 0; i_ < 16; ++i_) src[i_] = __builtin_amdgcn_exp2f(src[i_]); } \
        pa[j] = pack8(src, 0); pb[j] = pack8(src, 1); } while (0)
#define PV_TILE(j) do { const bf16x8 v0_ = lds_b128(vb + (j) * 4096), v1_ = lds_b128(vb + (j) * 4096 + 1024); o = MFMA(v0_, pa[j], o); o = MFMA(v1_, pb[j], o); } while (0)
        {
#pragma unroll
            for (int i = 0; i < 16; ++i) mg[i] = __builtin_amdgcn_exp2f(mg[i] - m);
#pragma unroll
            for (int i = 0; i < 16; ++i) {
                const int c = (i & 3) + 8 * (i >> 2) + 4 * h;
                s0[i] = c > r31 ? mg[i] : 0.f;
                mg[i] = c > r31 ? 0.f : mg[i];
            }
            pa[8] = pack8(mg, 0); pb[8] = pack8(mg, 1); pa[0] = pack8(s0, 0); pb[0] = pack8(s0, 1);
        }
        SBAR();
        SM_TILE(y2, 5); PV_TILE(8);
        EX_TILE(y0, 7); SBAR();
        SM_TILE(y0, 4);
        EX_TILE(y1, 6); SBAR();
        SM_TILE(y1, 3);
        EX_TILE(y2, 5); SBAR();
        SM_TILE(y2, 2);
        {
            const lptr lo = L + L_V + (j0 * 2 + hh) * 2048 + lane * 16;
            lds_w128(lo, hv[0]); lds_w128(lo + 1024, hv[1]); lds_w128(lo + 4096, hv[2]); lds_w128(lo + 4096 + 1024, hv[3]);
        }
        LBAR();
        PV_TILE(7); PV_TILE(6);
        EX_TILE(y0, 4); SBAR();
        SM_TILE(y0, 1); PV_TILE(5); PV_TILE(4);
        EX_TILE(y1, 3); SBAR();
        PV_TILE(0); PV_TILE(3);
        EX_TILE(y2, 2); SBAR();
        PV_TILE(2);
        EX_TILE(y0, 1); SBAR();
        PV_TILE(1);
    };
    if (__builtin_expect(r >= 2, 1)) attn(std::false_type{}); else attn(std::true_type{});
    {
        float l = o[12];
        auto rr = __builtin_amdgcn_permlane32_swap(__float_as_uint(l), __float_as_uint(l), false, false);
        l = __uint_as_float(rr[1]);
        const float inv = 1.0f / l;
#pragma unroll
        for (int i = 0; i < 16; ++i) o[i] *= inv;
        if (h == 1) {
#pragma unroll
            for (int i = 12; i < 16; ++i) o[i] = 0.f;
        }
        const unsigned go = WS_O + (unsigned)(((b * NTILE + 4 * r + t) * NH + 2 * g + hh) * 2048) + lane * 16;
        __builtin_amdgcn_raw_buffer_store_b128(pack8u(o, 0), rs, go, 0, AUX_SC1);
        __builtin_amdgcn_raw_buffer_store_b128(pack8u(o, 1), rs, go + 1024, 0, AUX_SC1);
        if (nimg == 2) {
#pragma unroll
            for (int i = 0; i < 7; ++i) glds16_nc(wo3 + 128 + (7 * w + i) * 8 * XROW, wob, lbase + L_WO1 + (7 * w + i) * 1024);
            WAITV(7);
        } else WAITV(0);
        flag_store(rs, WS_FO + wg * 128 + w * 8, lane, epoch);
    }

    const int tt = w & 1, nt = w >> 1;
    bf16x8 wfr[32];
    const int row0 = 4 * h + q;
    const lptr pw = L + L_WO0 + (nt >> 1) * (L_WO1 - L_WO0) + row0 * 128 + ((((nt & 1) << 6) + 32 * blk + 8 * pp) ^ wsw);
    const lptr pwl = pw + (h ? (444 - 15 * DH - 24 - 4) * 128 : 0);
    auto ld_wfr = [&]() {
#pragma unroll
        for (int hd = 0; hd < 16; ++hd)
#pragma unroll
            for (int s = 0; s < 2; ++s) {
                const int ro = (hd * DH + 16 * s) * 128;
                if (hd == 15 && s == 1) wfr[2 * hd + s] = tr_frag(pw + ro, pwl + ro + 1024);
                else wfr[2 * hd + s] = tr_frag(pw + ro, pw + ro + 1024);
            }
    };
    LBAR();
    if (nt < 2) ld_wfr();
    LBAR();
    wait_flags(ws, WS_FO + (unsigned)(((b * 16 + r) * 8 + w) * 128 + ((lane & 1) * 4 + 2 * (g >> 2) + ((lane >> 1) & 1)) * 8), lane < 4, epoch);
    {
        const char* ob = ws + WS_O;
        const unsigned go = (unsigned)(((b * NTILE + 2 * rb3) * NH + 2 * w) * 2048) + lane * 16;
#pragma unroll
        for (int i = 0; i < 4; ++i) {
            glds16_sc1(go + i * 1024, ob, lbase + w * 4096 + i * 1024);
            glds16_sc1(go + NH * 2048 + i * 1024, ob, lbase + 32768 + w * 4096 + i * 1024);
        }
    }
    WAITV_BAR(8);
    if (nt >= 2) ld_wfr();
    WAITV_BAR(0);
    f32x16 fin = {};
    {
        const lptr po = L + tt * 32768 + lane * 16;
        bf16x8 ofr[8];
#pragma unroll
        for (int k = 0; k < 8; ++k) ofr[k] = lds_b128(po + k * 1024);
        SBAR();
#pragma unroll
        for (int k = 0; k < 32; ++k) {
            fin = MFMA(wfr[k], ofr[k & 7], fin);
            if (k + 8 < 32) ofr[k & 7] = lds_b128(po + (k + 8) * 1024);
            SBAR();
        }
    }
    {
        const lptr ft = L + 98304 + (32 * tt + r31) * 272 + nt * 64 + h * 8;
#pragma unroll
        for (int e = 0; e < 4; ++e) {
            typedef unsigned u32x2 __attribute__((ext_vector_type(2)));
            const u32x2 v = {pk2(fin[4 * e], fin[4 * e + 1]), pk2(fin[4 * e + 2], fin[4 * e + 3])};
            *(LAS u32x2*)(ft + e * 16) = v;
        }
    }
    LBAR();
    {
        const int row = w * 8 + (lane >> 4), ch = lane & 15;
        char* op = out + (size_t)(64 * rb3 + row) * OROW + (b * D_ + 128 * ct) * 2 + ch * 16;
        const lptr fr = L + 98304 + row * 272 + ch * 16;
        const u32x4 v0 = *(const LAS u32x4*)fr, v1 = *(const LAS u32x4*)(fr + 4 * 272);
        const __amdgpu_buffer_rsrc_t ro = __builtin_amdgcn_make_buffer_rsrc(out, 0, S_ * OROW, 0x00020000);
        const unsigned oo = ch < 8 * nimg ? (unsigned)(op - out) : 0xF0000000u;
        __builtin_amdgcn_raw_buffer_store_b128(v0, ro, oo, 0, AUX_SC1);
        __builtin_amdgcn_raw_buffer_store_b128(v1, ro, oo + 4 * OROW, 0, AUX_SC1);
    }
    if (wg == 0 && w == 0) {
        const u64 t0 = __builtin_amdgcn_s_memrealtime();
        for (;;) {
            bool ok = true;
#pragma unroll
            for (int k = 0; k < 4; ++k) ok &= __hip_atomic_load((const GAS u64*)(ws + WS_FO + (k * 64 + lane) * 128), RLX_AGENT) == epoch;
            if (__all(ok)) break;
            __builtin_amdgcn_s_sleep(1);
            if (__builtin_amdgcn_s_memrealtime() - t0 > 2000000ull) break;
        }
        if (lane == 0) __hip_atomic_store((GAS u64*)(ws + WS_G), epoch, RLX_AGENT);
    }
}

extern "C" void kernel_launch(void* const* d_in, const int* in_sizes, int n_in, void* d_out, int out_size, void* d_ws, size_t ws_size,
                              hipStream_t stream) {
    (void)hipFuncSetAttribute((const void*)k_fused, hipFuncAttributeMaxDynamicSharedMemorySize, LDS_BYTES);
    k_fused<<<256, 512, LDS_BYTES, stream>>>((const char*)d_in[0], (const char*)d_in[1], (const char*)d_in[2], (const char*)d_in[3],
                                             (const char*)d_in[4], (const char*)d_in[5], (char*)d_out, (char*)d_ws);
}
```
